```python
import torch

def _softmax(x, axis=-1):
    return torch.softmax(x, dim=axis)
def _gelu(x):
    return torch.nn.functional.gelu(x, approximate="tanh")
def _silu(x):
    return torch.nn.functional.silu(x)
def _sigmoid(x):
    return torch.sigmoid(x)
def _layernorm(x, axis=-1, eps=1e-5):
    m = x.mean(dim=axis, keepdim=True)
    v = x.var(dim=axis, keepdim=True, unbiased=False)
    return (x - m) / torch.sqrt(v + eps)
def _scatter_add(tgt, idx, src, axis=0):
    return tgt.clone().index_add_(axis, idx, src)
def _segment_sum(src, seg, n):
    out = torch.zeros((n,) + src.shape[1:], dtype=src.dtype)
    return out.index_add_(0, seg, src)
def _pad(x, pw):
    flat = tuple(v for pair in reversed(pw) for v in pair)
    return torch.nn.functional.pad(x, flat)

kernel_name = 'standalone__low_rank_proj_linformer__sampling'

def setup_inputs():

    _w = (lambda a: (lambda t: t.to(torch.bfloat16) if t.is_floating_point() else t)(torch.as_tensor(a)))
    B, H, Sq, Skv, Dh, R = 16, 32, 1, 2048, 128, 64
    return {
        "q": _w((lambda *s: torch.randn(*s, dtype=torch.bfloat16) * 0.8)(B, H, Sq, Dh)),
        "k": _w((lambda *s: torch.randn(*s, dtype=torch.bfloat16) * 0.8)(B, H, Skv, Dh)),
        "v": _w((lambda *s: torch.randn(*s, dtype=torch.bfloat16) * 0.05)(B, H, Skv, Dh)),
        "e_proj": _w((lambda *s: torch.randn(*s, dtype=torch.bfloat16) * 0.8)(R, Skv) * (Skv ** -0.5)),
        "f_proj": _w((lambda *s: torch.randn(*s, dtype=torch.bfloat16) * 0.05)(R, Skv) * (Skv ** -0.5)),
    }

def reference(q, k, v, e_proj, f_proj):
    B, H, Sq, Dh = q.shape
    kp = torch.einsum("rn,bhnd->bhrd", e_proj, k)
    vp = torch.einsum("rn,bhnd->bhrd", f_proj, v)
    scores = torch.einsum("bhqd,bhrd->bhqr", q, kp) * (Dh ** -0.5)
    p = _softmax(scores, axis=-1)
    o = torch.einsum("bhqr,bhrd->bhqd", p, vp)
    return o
```

```cpp
#include <hip/hip_runtime.h>
#include <hip/hip_bf16.h>
#include <stdint.h>

typedef __attribute__((ext_vector_type(4))) unsigned u32x4;
typedef __attribute__((ext_vector_type(8))) __bf16 bf16x8;
typedef __attribute__((ext_vector_type(4))) float f32x4;

#define KV_AUX 2
#define INTERLEAVE 1

constexpr int NHEAD = 512, SKV = 2048, DH = 128, RK = 64;
constexpr int NWAVE = 4, TILE_ROWS = 32;
constexpr int TPW = SKV / TILE_ROWS / NWAVE;
constexpr unsigned HEAD_BYTES = SKV * DH * 2;
constexpr unsigned PROJ_BYTES = RK * SKV * 2;

template <int CTRL>
__device__ __forceinline__ float dpp(float x) {
  return __builtin_bit_cast(float, __builtin_amdgcn_mov_dpp(__builtin_bit_cast(int, x), CTRL, 0xf, 0xf, true));
}
constexpr int XOR1 = 0xB1, XOR2 = 0x4E, XOR7 = 0x141, XOR8 = 0x128, ROR4 = 0x124;

__device__ __forceinline__ float xrow_sum(float x) {
  auto s = __builtin_amdgcn_permlane16_swap(__float_as_uint(x), __float_as_uint(x), false, false);
  x = __uint_as_float(s[0]) + __uint_as_float(s[1]);
  auto t = __builtin_amdgcn_permlane32_swap(__float_as_uint(x), __float_as_uint(x), false, false);
  return __uint_as_float(t[0]) + __uint_as_float(t[1]);
}
__device__ __forceinline__ float xrow_max(float x) {
  auto s = __builtin_amdgcn_permlane16_swap(__float_as_uint(x), __float_as_uint(x), false, false);
  x = fmaxf(__uint_as_float(s[0]), __uint_as_float(s[1]));
  auto t = __builtin_amdgcn_permlane32_swap(__float_as_uint(x), __float_as_uint(x), false, false);
  return fmaxf(__uint_as_float(t[0]), __uint_as_float(t[1]));
}

struct Tile {
  u32x4 d[8];
  u32x4 p[4];
};

__device__ __forceinline__ void issue_tile(Tile& t, const void* dbase, const void* pbase, unsigned tile, unsigned voff_d,
                                           unsigned voff_p) {
  __amdgpu_buffer_rsrc_t rd = __builtin_amdgcn_make_buffer_rsrc((void*)dbase, 0, HEAD_BYTES, 0x00020000);
  __amdgpu_buffer_rsrc_t rp = __builtin_amdgcn_make_buffer_rsrc((void*)pbase, 0, PROJ_BYTES, 0x00020000);
  const unsigned so_p = tile * (TILE_ROWS * 2);
  const unsigned so_d = tile * (TILE_ROWS * DH * 2);
#pragma unroll
  for (int b = 0; b < 4; ++b) t.p[b] = __builtin_amdgcn_raw_buffer_load_b128(rp, voff_p, so_p + b * (16 * SKV * 2), 0);
#pragma unroll
  for (int i = 0; i < 8; ++i) t.d[i] = __builtin_amdgcn_raw_buffer_load_b128(rd, voff_d + i * (DH * 2), so_d, KV_AUX);
}

__device__ __forceinline__ void compute_tile(const Tile& t, f32x4 (&acc)[4][8]) {
#pragma unroll
  for (int j = 0; j < 8; ++j) {
    u32x4 bf;
#pragma unroll
    for (int m = 0; m < 4; ++m)
      bf[m] = __builtin_amdgcn_perm(t.d[2 * m + 1][j >> 1], t.d[2 * m][j >> 1], (j & 1) ? 0x07060302u : 0x05040100u);
    const bf16x8 B = __builtin_bit_cast(bf16x8, bf);
#pragma unroll
    for (int b = 0; b < 4; ++b)
      acc[b][j] = __builtin_amdgcn_mfma_f32_16x16x32_bf16(__builtin_bit_cast(bf16x8, t.p[b]), B, acc[b][j], 0, 0, 0);
  }
}

__global__ void __launch_bounds__(256, 2)
linformer_decode_kernel(const uint16_t* __restrict__ q, const uint16_t* __restrict__ k, const uint16_t* __restrict__ v,
                        const uint16_t* __restrict__ e, const uint16_t* __restrict__ f, uint16_t* __restrict__ out) {
  __shared__ __attribute__((aligned(16))) float lds[NWAVE * RK + NWAVE * RK + NWAVE * DH];
  float* lds_s = lds;
  float* lds_p = lds + NWAVE * RK;
  float* lds_o = lds_p + NWAVE * RK;

  const int head = blockIdx.x;
  const int lane = threadIdx.x & 63, wave = __builtin_amdgcn_readfirstlane(threadIdx.x >> 6);
  const int g = lane >> 4, c = lane & 15;
  const char* kb = (const char*)k + (size_t)head * HEAD_BYTES;
  const char* vb = (const char*)v + (size_t)head * HEAD_BYTES;
  const unsigned voff_d = g * (8 * DH * 2) + c * 16;
  const unsigned voff_p = c * (SKV * 2) + g * 16;

#define TILE_OF(s) ((((s) & (TPW - 1)) * NWAVE) + wave)

  Tile t0, t1;
  issue_tile(t0, kb, e, TILE_OF(0), voff_d, voff_p);
  issue_tile(t1, kb, e, TILE_OF(1), voff_d, voff_p);
  const u32x4 qv = *reinterpret_cast<const u32x4*>(q + head * DH + c * 8);

  f32x4 acc[4][8];
#pragma unroll
  for (int b = 0; b < 4; ++b)
#pragma unroll
    for (int j = 0; j < 8; ++j) acc[b][j] = f32x4{0.f, 0.f, 0.f, 0.f};

#pragma unroll 1
  for (int s = 0; s < 2 * TPW - 2; s += 2) {
    const bool nextv = (s + 2) >= TPW;
    const void* db = nextv ? (const void*)vb : (const void*)kb;
    const void* pb = nextv ? (const void*)f : (const void*)e;
    compute_tile(t0, acc);
    issue_tile(t0, db, pb, TILE_OF(s + 2), voff_d, voff_p);
    compute_tile(t1, acc);
    issue_tile(t1, db, pb, TILE_OF(s + 3), voff_d, voff_p);
    if (s == TPW - 2) {
      float qf[8];
#pragma unroll
      for (int j = 0; j < 4; ++j) {
        qf[2 * j] = __uint_as_float(qv[j] << 16);
        qf[2 * j + 1] = __uint_as_float(qv[j] & 0xffff0000u);
      }
      float x[16];
#pragma unroll
      for (int b = 0; b < 4; ++b)
#pragma unroll
        for (int m = 0; m < 4; ++m) {
          float a = 0.f;
#pragma unroll
          for (int j = 0; j < 8; ++j) a = fmaf(qf[j], acc[b][j][m], a);
          x[b * 4 + m] = a;
        }
#define TR_STEP(HALF, CTRL, BIT)                                              \
  _Pragma("unroll") for (int h = 0; h < (HALF); ++h) {                        \
    const float lo_ = x[h], hi_ = x[h + (HALF)];                              \
    const float t1_ = lo_ + dpp<CTRL>(lo_), t2_ = hi_ + dpp<CTRL>(hi_);      \
    x[h] = (c & (BIT)) ? t2_ : t1_;                                           \
  }
      TR_STEP(8, XOR8, 8) TR_STEP(4, XOR7, 4) TR_STEP(2, XOR2, 2) TR_STEP(1, XOR1, 1)
#undef TR_STEP
      lds_s[wave * RK + 16 * (c >> 2) + 4 * g + (c & 3)] = x[0];
#pragma unroll
      for (int b = 0; b < 4; ++b)
#pragma unroll
        for (int j = 0; j < 8; ++j) acc[b][j] = f32x4{0.f, 0.f, 0.f, 0.f};
    }
  }
  __syncthreads();
  {
    float sc = (lds_s[lane] + lds_s[RK + lane]) + (lds_s[2 * RK + lane] + lds_s[3 * RK + lane]);
    sc *= 0.08838834764831845f;
    float mx = sc;
    mx = fmaxf(mx, dpp<XOR1>(mx));
    mx = fmaxf(mx, dpp<XOR2>(mx));
    mx = fmaxf(mx, dpp<ROR4>(mx));
    mx = fmaxf(mx, dpp<XOR8>(mx));
    mx = xrow_max(mx);
    const float pe = __expf(sc - mx);
    float sum = pe;
    sum += dpp<XOR1>(sum);
    sum += dpp<XOR2>(sum);
    sum += dpp<ROR4>(sum);
    sum += dpp<XOR8>(sum);
    sum = xrow_sum(sum);
    lds_p[wave * RK + lane] = pe / sum;
  }
  __builtin_amdgcn_fence(__ATOMIC_RELEASE, "wavefront");
  __builtin_amdgcn_wave_barrier();
  __builtin_amdgcn_fence(__ATOMIC_ACQUIRE, "wavefront");
  f32x4 pr[4];
#pragma unroll
  for (int b = 0; b < 4; ++b) pr[b] = *reinterpret_cast<const f32x4*>(lds_p + wave * RK + 16 * b + 4 * g);

  compute_tile(t0, acc);
  compute_tile(t1, acc);

  float o[8];
#pragma unroll
  for (int j = 0; j < 8; ++j) {
    float a = 0.f;
#pragma unroll
    for (int b = 0; b < 4; ++b)
#pragma unroll
      for (int m = 0; m < 4; ++m) a = fmaf(pr[b][m], acc[b][j][m], a);
    o[j] = xrow_sum(a);
  }
  if (g == 0) {
    *reinterpret_cast<f32x4*>(lds_o + wave * DH + 8 * c) = f32x4{o[0], o[1], o[2], o[3]};
    *reinterpret_cast<f32x4*>(lds_o + wave * DH + 8 * c + 4) = f32x4{o[4], o[5], o[6], o[7]};
  }
  __syncthreads();
  if (threadIdx.x < 64) {
    const int d0 = 2 * lane;
    float r0 = (lds_o[d0] + lds_o[DH + d0]) + (lds_o[2 * DH + d0] + lds_o[3 * DH + d0]);
    float r1 = (lds_o[d0 + 1] + lds_o[DH + d0 + 1]) + (lds_o[2 * DH + d0 + 1] + lds_o[3 * DH + d0 + 1]);
    __hip_bfloat162 pk;
    pk.x = __float2bfloat16(r0);
    pk.y = __float2bfloat16(r1);
    *reinterpret_cast<__hip_bfloat162*>(out + head * DH + d0) = pk;
  }
}

extern "C" void kernel_launch(void* const* d_in, const int* in_sizes, int n_in, void* d_out, int out_size, void* d_ws,
                              size_t ws_size, hipStream_t stream) {
  const uint16_t* q = (const uint16_t*)d_in[0];
  const uint16_t* k = (const uint16_t*)d_in[1];
  const uint16_t* v = (const uint16_t*)d_in[2];
  const uint16_t* e = (const uint16_t*)d_in[3];
  const uint16_t* f = (const uint16_t*)d_in[4];
  linformer_decode_kernel<<<NHEAD, 256, 0, stream>>>(q, k, v, e, f, (uint16_t*)d_out);
}
```

```cpp
#include <hip/hip_runtime.h>
#include <hip/hip_bf16.h>
#include <stdint.h>

typedef __attribute__((ext_vector_type(4))) unsigned u32x4;
typedef __attribute__((ext_vector_type(8))) __bf16 bf16x8;
typedef __attribute__((ext_vector_type(4))) float f32x4;

#define KV_AUX 2
#define INTERLEAVE 1

#define SB() __builtin_amdgcn_sched_barrier(0)
constexpr int NHEAD = 512, SKV = 2048, DH = 128, RK = 64;
constexpr int NWAVE = 4, TILE_ROWS = 32;
constexpr int TPW = SKV / TILE_ROWS / NWAVE;
constexpr unsigned HEAD_BYTES = SKV * DH * 2;
constexpr unsigned PROJ_BYTES = RK * SKV * 2;

template <int CTRL>
__device__ __forceinline__ float dpp(float x) {
  return __builtin_bit_cast(float, __builtin_amdgcn_mov_dpp(__builtin_bit_cast(int, x), CTRL, 0xf, 0xf, true));
}
constexpr int XOR1 = 0xB1, XOR2 = 0x4E, XOR7 = 0x141, XOR8 = 0x128, ROR4 = 0x124;

__device__ __forceinline__ float xrow_sum(float x) {
  auto s = __builtin_amdgcn_permlane16_swap(__float_as_uint(x), __float_as_uint(x), false, false);
  x = __uint_as_float(s[0]) + __uint_as_float(s[1]);
  auto t = __builtin_amdgcn_permlane32_swap(__float_as_uint(x), __float_as_uint(x), false, false);
  return __uint_as_float(t[0]) + __uint_as_float(t[1]);
}
__device__ __forceinline__ float xrow_max(float x) {
  auto s = __builtin_amdgcn_permlane16_swap(__float_as_uint(x), __float_as_uint(x), false, false);
  x = fmaxf(__uint_as_float(s[0]), __uint_as_float(s[1]));
  auto t = __builtin_amdgcn_permlane32_swap(__float_as_uint(x), __float_as_uint(x), false, false);
  return fmaxf(__uint_as_float(t[0]), __uint_as_float(t[1]));
}

struct Tile {
  u32x4 d[8];
  u32x4 p[4];
};

__device__ __forceinline__ void issue_tile(Tile& t, const void* dbase, const void* pbase, unsigned tile, unsigned voff_d,
                                           unsigned voff_p) {
  __amdgpu_buffer_rsrc_t rd = __builtin_amdgcn_make_buffer_rsrc((void*)dbase, 0, HEAD_BYTES, 0x00020000);
  __amdgpu_buffer_rsrc_t rp = __builtin_amdgcn_make_buffer_rsrc((void*)pbase, 0, PROJ_BYTES, 0x00020000);
  const unsigned so_p = tile * (TILE_ROWS * 2);
  const unsigned so_d = tile * (TILE_ROWS * DH * 2);
#pragma unroll
  for (int b = 0; b < 4; ++b) t.p[b] = __builtin_amdgcn_raw_buffer_load_b128(rp, voff_p, so_p + b * (16 * SKV * 2), 0);
#pragma unroll
  for (int i = 0; i < 8; ++i) t.d[i] = __builtin_amdgcn_raw_buffer_load_b128(rd, voff_d + i * (DH * 2), so_d, KV_AUX);
}

__device__ __forceinline__ void compute_tile(const Tile& t, f32x4 (&acc)[4][8]) {
#pragma unroll
  for (int j = 0; j < 8; ++j) {
    u32x4 bf;
#pragma unroll
    for (int m = 0; m < 4; ++m)
      bf[m] = __builtin_amdgcn_perm(t.d[2 * m + 1][j >> 1], t.d[2 * m][j >> 1], (j & 1) ? 0x07060302u : 0x05040100u);
    const bf16x8 B = __builtin_bit_cast(bf16x8, bf);
#pragma unroll
    for (int b = 0; b < 4; ++b)
      acc[b][j] = __builtin_amdgcn_mfma_f32_16x16x32_bf16(__builtin_bit_cast(bf16x8, t.p[b]), B, acc[b][j], 0, 0, 0);
  }
}

__global__ void __launch_bounds__(256, 2)
linformer_decode_kernel(const uint16_t* __restrict__ q, const uint16_t* __restrict__ k, const uint16_t* __restrict__ v,
                        const uint16_t* __restrict__ e, const uint16_t* __restrict__ f, uint16_t* __restrict__ out) {
  __shared__ __attribute__((aligned(16))) float lds[NWAVE * RK + NWAVE * RK + NWAVE * DH];
  float* lds_s = lds;
  float* lds_p = lds + NWAVE * RK;
  float* lds_o = lds_p + NWAVE * RK;

  const int head = blockIdx.x;
  const int lane = threadIdx.x & 63, wave = __builtin_amdgcn_readfirstlane(threadIdx.x >> 6);
  const int g = lane >> 4, c = lane & 15;
  const char* kb = (const char*)k + (size_t)head * HEAD_BYTES;
  const char* vb = (const char*)v + (size_t)head * HEAD_BYTES;
  const unsigned voff_d = g * (8 * DH * 2) + c * 16;
  const unsigned voff_p = c * (SKV * 2) + g * 16;

#define TILE_OF(s) ((((s) & (TPW - 1)) * NWAVE) + wave)

  Tile t0, t1;
  issue_tile(t0, kb, e, TILE_OF(0), voff_d, voff_p);
  issue_tile(t1, kb, e, TILE_OF(1), voff_d, voff_p);
  SB();
  u32x4 qv = *reinterpret_cast<const u32x4*>(q + head * DH + c * 8);
  SB();

  f32x4 acc[4][8];
#pragma unroll
  for (int b = 0; b < 4; ++b)
#pragma unroll
    for (int j = 0; j < 8; ++j) acc[b][j] = f32x4{0.f, 0.f, 0.f, 0.f};

#pragma unroll 1
  for (int s = 0; s < 2 * TPW - 2; s += 2) {
    const bool nextv = (s + 2) >= TPW;
    const void* db = nextv ? (const void*)vb : (const void*)kb;
    const void* pb = nextv ? (const void*)f : (const void*)e;
    compute_tile(t0, acc);
    SB();
    issue_tile(t0, db, pb, TILE_OF(s + 2), voff_d, voff_p);
    SB();
    compute_tile(t1, acc);
    SB();
    issue_tile(t1, db, pb, TILE_OF(s + 3), voff_d, voff_p);
    SB();
    if (s == TPW - 2) {
      asm volatile("" : "+v"(qv));
      float qf[8];
#pragma unroll
      for (int j = 0; j < 4; ++j) {
        qf[2 * j] = __uint_as_float(qv[j] << 16);
        qf[2 * j + 1] = __uint_as_float(qv[j] & 0xffff0000u);
      }
      float x[16];
#pragma unroll
      for (int b = 0; b < 4; ++b)
#pragma unroll
        for (int m = 0; m < 4; ++m) {
          float a = 0.f;
#pragma unroll
          for (int j = 0; j < 8; ++j) a = fmaf(qf[j], acc[b][j][m], a);
          x[b * 4 + m] = a;
        }
#define TR_STEP(HALF, CTRL, BIT)                                              \
  _Pragma("unroll") for (int h = 0; h < (HALF); ++h) {                        \
    const float lo_ = x[h], hi_ = x[h + (HALF)];                              \
    const float t1_ = lo_ + dpp<CTRL>(lo_), t2_ = hi_ + dpp<CTRL>(hi_);      \
    x[h] = (c & (BIT)) ? t2_ : t1_;                                           \
  }
      TR_STEP(8, XOR8, 8) TR_STEP(4, XOR7, 4) TR_STEP(2, XOR2, 2) TR_STEP(1, XOR1, 1)
#undef TR_STEP
      lds_s[wave * RK + 16 * (c >> 2) + 4 * g + (c & 3)] = x[0];
#pragma unroll
      for (int b = 0; b < 4; ++b)
#pragma unroll
        for (int j = 0; j < 8; ++j) acc[b][j] = f32x4{0.f, 0.f, 0.f, 0.f};
    }
  }
  __syncthreads();
  {
    float sc = (lds_s[lane] + lds_s[RK + lane]) + (lds_s[2 * RK + lane] + lds_s[3 * RK + lane]);
    sc *= 0.08838834764831845f;
    float mx = sc;
    mx = fmaxf(mx, dpp<XOR1>(mx));
    mx = fmaxf(mx, dpp<XOR2>(mx));
    mx = fmaxf(mx, dpp<ROR4>(mx));
    mx = fmaxf(mx, dpp<XOR8>(mx));
    mx = xrow_max(mx);
    const float pe = __expf(sc - mx);
    float sum = pe;
    sum += dpp<XOR1>(sum);
    sum += dpp<XOR2>(sum);
    sum += dpp<ROR4>(sum);
    sum += dpp<XOR8>(sum);
    sum = xrow_sum(sum);
    lds_p[wave * RK + lane] = pe / sum;
  }
  __builtin_amdgcn_fence(__ATOMIC_RELEASE, "wavefront");
  __builtin_amdgcn_wave_barrier();
  __builtin_amdgcn_fence(__ATOMIC_ACQUIRE, "wavefront");
  f32x4 pr[4];
#pragma unroll
  for (int b = 0; b < 4; ++b) pr[b] = *reinterpret_cast<const f32x4*>(lds_p + wave * RK + 16 * b + 4 * g);

  SB();
  compute_tile(t0, acc);
  SB();
  compute_tile(t1, acc);

  float o[8];
#pragma unroll
  for (int j = 0; j < 8; ++j) {
    float a = 0.f;
#pragma unroll
    for (int b = 0; b < 4; ++b)
#pragma unroll
      for (int m = 0; m < 4; ++m) a = fmaf(pr[b][m], acc[b][j][m], a);
    o[j] = xrow_sum(a);
  }
  if (g == 0) {
    *reinterpret_cast<f32x4*>(lds_o + wave * DH + 8 * c) = f32x4{o[0], o[1], o[2], o[3]};
    *reinterpret_cast<f32x4*>(lds_o + wave * DH + 8 * c + 4) = f32x4{o[4], o[5], o[6], o[7]};
  }
  __syncthreads();
  if (threadIdx.x < 64) {
    const int d0 = 2 * lane;
    float r0 = (lds_o[d0] + lds_o[DH + d0]) + (lds_o[2 * DH + d0] + lds_o[3 * DH + d0]);
    float r1 = (lds_o[d0 + 1] + lds_o[DH + d0 + 1]) + (lds_o[2 * DH + d0 + 1] + lds_o[3 * DH + d0 + 1]);
    __hip_bfloat162 pk;
    pk.x = __float2bfloat16(r0);
    pk.y = __float2bfloat16(r1);
    *reinterpret_cast<__hip_bfloat162*>(out + head * DH + d0) = pk;
  }
}

extern "C" void kernel_launch(void* const* d_in, const int* in_sizes, int n_in, void* d_out, int out_size, void* d_ws,
                              size_t ws_size, hipStream_t stream) {
  const uint16_t* q = (const uint16_t*)d_in[0];
  const uint16_t* k = (const uint16_t*)d_in[1];
  const uint16_t* v = (const uint16_t*)d_in[2];
  const uint16_t* e = (const uint16_t*)d_in[3];
  const uint16_t* f = (const uint16_t*)d_in[4];
  linformer_decode_kernel<<<NHEAD, 256, 0, stream>>>(q, k, v, e, f, (uint16_t*)d_out);
}
```

```cpp
#include <hip/hip_runtime.h>
#include <hip/hip_bf16.h>
#include <stdint.h>

typedef __attribute__((ext_vector_type(4))) unsigned u32x4;
typedef __attribute__((ext_vector_type(8))) __bf16 bf16x8;
typedef __attribute__((ext_vector_type(4))) float f32x4;

#define KV_AUX 2
#define INTERLEAVE 1

#define SB() __builtin_amdgcn_sched_barrier(0)
constexpr int NHEAD = 512, SKV = 2048, DH = 128, RK = 64;
constexpr int NWAVE = 4, TILE_ROWS = 32;
constexpr int TPW = SKV / TILE_ROWS / NWAVE;
constexpr unsigned HEAD_BYTES = SKV * DH * 2;
constexpr unsigned PROJ_BYTES = RK * SKV * 2;

template <int CTRL>
__device__ __forceinline__ float dpp(float x) {
  return __builtin_bit_cast(float, __builtin_amdgcn_mov_dpp(__builtin_bit_cast(int, x), CTRL, 0xf, 0xf, true));
}
constexpr int XOR1 = 0xB1, XOR2 = 0x4E, XOR7 = 0x141, XOR8 = 0x128, ROR4 = 0x124;

__device__ __forceinline__ float xrow_sum(float x) {
  auto s = __builtin_amdgcn_permlane16_swap(__float_as_uint(x), __float_as_uint(x), false, false);
  x = __uint_as_float(s[0]) + __uint_as_float(s[1]);
  auto t = __builtin_amdgcn_permlane32_swap(__float_as_uint(x), __float_as_uint(x), false, false);
  return __uint_as_float(t[0]) + __uint_as_float(t[1]);
}
__device__ __forceinline__ float xrow_max(float x) {
  auto s = __builtin_amdgcn_permlane16_swap(__float_as_uint(x), __float_as_uint(x), false, false);
  x = fmaxf(__uint_as_float(s[0]), __uint_as_float(s[1]));
  auto t = __builtin_amdgcn_permlane32_swap(__float_as_uint(x), __float_as_uint(x), false, false);
  return fmaxf(__uint_as_float(t[0]), __uint_as_float(t[1]));
}

#define PMODE 1
struct Tile {
  u32x4 d[8];
};
struct Proj {
  u32x4 x[4], y[4];
};

__device__ __forceinline__ void issue_tile(Tile& t, const void* dbase, unsigned tile, unsigned voff_d) {
  __amdgpu_buffer_rsrc_t rd = __builtin_amdgcn_make_buffer_rsrc((void*)dbase, 0, HEAD_BYTES, 0x00020000);
  const unsigned so_d = tile * (TILE_ROWS * DH * 2);
#pragma unroll
  for (int i = 0; i < 8; ++i) t.d[i] = __builtin_amdgcn_raw_buffer_load_b128(rd, voff_d + i * (DH * 2), so_d, KV_AUX);
}
__device__ __forceinline__ void issue_proj(Proj& p, const void* pbase, unsigned pair, unsigned voff_x, unsigned voff_y) {
  __amdgpu_buffer_rsrc_t rp = __builtin_amdgcn_make_buffer_rsrc((void*)pbase, 0, PROJ_BYTES, 0x00020000);
  const unsigned so_p = pair * (2 * TILE_ROWS * 2);
#pragma unroll
  for (int b = 0; b < 4; ++b) {
    p.x[b] = __builtin_amdgcn_raw_buffer_load_b128(rp, voff_x, so_p + b * (16 * SKV * 2), 0);
    p.y[b] = __builtin_amdgcn_raw_buffer_load_b128(rp, voff_y, so_p + b * (16 * SKV * 2), 0);
  }
}
#define SPLIT 0
template <int H>
__device__ __forceinline__ void issue_both(Tile& t, Proj& p, const void* dbase, const void* pbase, unsigned pair,
                                           unsigned voff_d, unsigned voff_x, unsigned voff_y) {
  __amdgpu_buffer_rsrc_t rd = __builtin_amdgcn_make_buffer_rsrc((void*)dbase, 0, HEAD_BYTES, 0x00020000);
  __amdgpu_buffer_rsrc_t rp = __builtin_amdgcn_make_buffer_rsrc((void*)pbase, 0, PROJ_BYTES, 0x00020000);
  const unsigned so_d = (2 * pair + H) * (TILE_ROWS * DH * 2);
  const unsigned so_p = pair * (2 * TILE_ROWS * 2);
#pragma unroll
  for (int i = 0; i < 4; ++i) {
    const int b = 2 * H + (i >> 1);
    if (i & 1) p.y[b] = __builtin_amdgcn_raw_buffer_load_b128(rp, voff_y, so_p + b * (16 * SKV * 2), 0);
    else p.x[b] = __builtin_amdgcn_raw_buffer_load_b128(rp, voff_x, so_p + b * (16 * SKV * 2), 0);
    SB();
    if (H == 0 || SPLIT == 2) {
      t.d[2 * i] = __builtin_amdgcn_raw_buffer_load_b128(rd, voff_d + (2 * i) * (DH * 2), so_d, KV_AUX);
      t.d[2 * i + 1] = __builtin_amdgcn_raw_buffer_load_b128(rd, voff_d + (2 * i + 1) * (DH * 2), so_d, KV_AUX);
    } else {
      t.d[i] = __builtin_amdgcn_raw_buffer_load_b128(rd, voff_d + (i) * (DH * 2), so_d, KV_AUX);
    }
    SB();
  }
  if (H == 1 && SPLIT == 3) {
#pragma unroll
    for (int i = 4; i < 8; ++i) t.d[i] = __builtin_amdgcn_raw_buffer_load_b128(rd, voff_d + i * (DH * 2), so_d, KV_AUX);
  }
}
__device__ __forceinline__ void fix_proj(Proj& p) {
#pragma unroll
  for (int b = 0; b < 4; ++b)
#pragma unroll
    for (int m = 0; m < 4; ++m) {
      const int X = p.x[b][m], Y = p.y[b][m];
      int T = __builtin_amdgcn_mov_dpp(X, 0x128, 0xf, 0xf, true);
      T = __builtin_amdgcn_update_dpp(T, Y, 0x128, 0xf, 0xc, false);
      const int A = __builtin_amdgcn_update_dpp(X, Y, 0xE4, 0xf, 0xc, false);
      p.x[b][m] = A;
      p.y[b][m] = T;
    }
}

__device__ __forceinline__ void compute_tile(const Tile& t, const u32x4 (&p)[4], f32x4 (&acc)[4][8]) {
#pragma unroll
  for (int j = 0; j < 8; ++j) {
    u32x4 bf;
#pragma unroll
    for (int m = 0; m < 4; ++m)
      bf[m] = __builtin_amdgcn_perm(t.d[2 * m + 1][j >> 1], t.d[2 * m][j >> 1], (j & 1) ? 0x07060302u : 0x05040100u);
    const bf16x8 B = __builtin_bit_cast(bf16x8, bf);
#pragma unroll
    for (int b = 0; b < 4; ++b)
      acc[b][j] = __builtin_amdgcn_mfma_f32_16x16x32_bf16(__builtin_bit_cast(bf16x8, p[b]), B, acc[b][j], 0, 0, 0);
  }
}

__global__ void __launch_bounds__(256, 2)
linformer_decode_kernel(const uint16_t* __restrict__ q, const uint16_t* __restrict__ k, const uint16_t* __restrict__ v,
                        const uint16_t* __restrict__ e, const uint16_t* __restrict__ f, uint16_t* __restrict__ out) {
  __shared__ __attribute__((aligned(16))) float lds[NWAVE * RK + NWAVE * RK + NWAVE * DH];
  float* lds_s = lds;
  float* lds_p = lds + NWAVE * RK;
  float* lds_o = lds_p + NWAVE * RK;

  const int head = blockIdx.x;
  const int lane = threadIdx.x & 63, wave = __builtin_amdgcn_readfirstlane(threadIdx.x >> 6);
  const int g = lane >> 4, c = lane & 15;
  const char* kb = (const char*)k + (size_t)head * HEAD_BYTES;
  const char* vb = (const char*)v + (size_t)head * HEAD_BYTES;
  const unsigned voff_d = g * (8 * DH * 2) + c * 16;
  const unsigned voff_x = (c & 7) * (SKV * 2) + (g + 4 * (c >> 3)) * 16;
  const unsigned voff_y = (8 + (c & 7)) * (SKV * 2) + (g + 4 * (1 - (c >> 3))) * 16;
#define PAIR_OF(s) (((((s) & (TPW - 1)) >> 1) * NWAVE) + wave)

  Tile t0, t1;
  Proj pj;
  issue_proj(pj, e, PAIR_OF(0), voff_x, voff_y);
  issue_tile(t0, kb, 2 * PAIR_OF(0), voff_d);
  issue_tile(t1, kb, 2 * PAIR_OF(0) + 1, voff_d);
  SB();
  u32x4 qv = *reinterpret_cast<const u32x4*>(q + head * DH + c * 8);
  SB();

  f32x4 acc[4][8];
#pragma unroll
  for (int b = 0; b < 4; ++b)
#pragma unroll
    for (int j = 0; j < 8; ++j) acc[b][j] = f32x4{0.f, 0.f, 0.f, 0.f};

#pragma unroll 1
  for (int s = 0; s < 2 * TPW - 2; s += 2) {
    const bool nextv = (s + 2) >= TPW;
    const void* db = nextv ? (const void*)vb : (const void*)kb;
    const void* pb = nextv ? (const void*)f : (const void*)e;
    fix_proj(pj);
    compute_tile(t0, pj.x, acc);
    SB();
    issue_tile(t0, db, 2 * PAIR_OF(s + 2), voff_d);
    SB();
    compute_tile(t1, pj.y, acc);
    SB();
    issue_proj(pj, pb, PAIR_OF(s + 2), voff_x, voff_y);
    issue_tile(t1, db, 2 * PAIR_OF(s + 2) + 1, voff_d);
    SB();
    if (s == TPW - 2) {
      asm volatile("" : "+v"(qv));
      float qf[8];
#pragma unroll
      for (int j = 0; j < 4; ++j) {
        qf[2 * j] = __uint_as_float(qv[j] << 16);
        qf[2 * j + 1] = __uint_as_float(qv[j] & 0xffff0000u);
      }
      float x[16];
#pragma unroll
      for (int b = 0; b < 4; ++b)
#pragma unroll
        for (int m = 0; m < 4; ++m) {
          float a = 0.f;
#pragma unroll
          for (int j = 0; j < 8; ++j) a = fmaf(qf[j], acc[b][j][m], a);
          x[b * 4 + m] = a;
        }
#define TR_STEP(HALF, CTRL, BIT)                                              \
  _Pragma("unroll") for (int h = 0; h < (HALF); ++h) {                        \
    const float lo_ = x[h], hi_ = x[h + (HALF)];                              \
    const float t1_ = lo_ + dpp<CTRL>(lo_), t2_ = hi_ + dpp<CTRL>(hi_);      \
    x[h] = (c & (BIT)) ? t2_ : t1_;                                           \
  }
      TR_STEP(8, XOR8, 8) TR_STEP(4, XOR7, 4) TR_STEP(2, XOR2, 2) TR_STEP(1, XOR1, 1)
#undef TR_STEP
      lds_s[wave * RK + 16 * (c >> 2) + 4 * g + (c & 3)] = x[0];
#pragma unroll
      for (int b = 0; b < 4; ++b)
#pragma unroll
        for (int j = 0; j < 8; ++j) acc[b][j] = f32x4{0.f, 0.f, 0.f, 0.f};
    }
  }
  __syncthreads();
  {
    float sc = (lds_s[lane] + lds_s[RK + lane]) + (lds_s[2 * RK + lane] + lds_s[3 * RK + lane]);
    sc *= 0.08838834764831845f;
    float mx = sc;
    mx = fmaxf(mx, dpp<XOR1>(mx));
    mx = fmaxf(mx, dpp<XOR2>(mx));
    mx = fmaxf(mx, dpp<ROR4>(mx));
    mx = fmaxf(mx, dpp<XOR8>(mx));
    mx = xrow_max(mx);
    const float pe = __expf(sc - mx);
    float sum = pe;
    sum += dpp<XOR1>(sum);
    sum += dpp<XOR2>(sum);
    sum += dpp<ROR4>(sum);
    sum += dpp<XOR8>(sum);
    sum = xrow_sum(sum);
    lds_p[wave * RK + lane] = pe / sum;
  }
  __builtin_amdgcn_fence(__ATOMIC_RELEASE, "wavefront");
  __builtin_amdgcn_wave_barrier();
  __builtin_amdgcn_fence(__ATOMIC_ACQUIRE, "wavefront");
  f32x4 pr[4];
#pragma unroll
  for (int b = 0; b < 4; ++b) pr[b] = *reinterpret_cast<const f32x4*>(lds_p + wave * RK + 16 * b + 4 * g);

  SB();
  fix_proj(pj);
  compute_tile(t0, pj.x, acc);
  SB();
  compute_tile(t1, pj.y, acc);

  float o[8];
#pragma unroll
  for (int j = 0; j < 8; ++j) {
    float a = 0.f;
#pragma unroll
    for (int b = 0; b < 4; ++b)
#pragma unroll
      for (int m = 0; m < 4; ++m) a = fmaf(pr[b][m], acc[b][j][m], a);
    o[j] = xrow_sum(a);
  }
  if (g == 0) {
    *reinterpret_cast<f32x4*>(lds_o + wave * DH + 8 * c) = f32x4{o[0], o[1], o[2], o[3]};
    *reinterpret_cast<f32x4*>(lds_o + wave * DH + 8 * c + 4) = f32x4{o[4], o[5], o[6], o[7]};
  }
  __syncthreads();
  if (threadIdx.x < 64) {
    const int d0 = 2 * lane;
    float r0 = (lds_o[d0] + lds_o[DH + d0]) + (lds_o[2 * DH + d0] + lds_o[3 * DH + d0]);
    float r1 = (lds_o[d0 + 1] + lds_o[DH + d0 + 1]) + (lds_o[2 * DH + d0 + 1] + lds_o[3 * DH + d0 + 1]);
    __hip_bfloat162 pk;
    pk.x = __float2bfloat16(r0);
    pk.y = __float2bfloat16(r1);
    *reinterpret_cast<__hip_bfloat162*>(out + head * DH + d0) = pk;
  }
}

extern "C" void kernel_launch(void* const* d_in, const int* in_sizes, int n_in, void* d_out, int out_size, void* d_ws,
                              size_t ws_size, hipStream_t stream) {
  const uint16_t* q = (const uint16_t*)d_in[0];
  const uint16_t* k = (const uint16_t*)d_in[1];
  const uint16_t* v = (const uint16_t*)d_in[2];
  const uint16_t* e = (const uint16_t*)d_in[3];
  const uint16_t* f = (const uint16_t*)d_in[4];
  linformer_decode_kernel<<<NHEAD, 256, 0, stream>>>(q, k, v, e, f, (uint16_t*)d_out);
}
```

```cpp
#include <hip/hip_runtime.h>
#include <hip/hip_bf16.h>
#include <stdint.h>

typedef __attribute__((ext_vector_type(4))) unsigned u32x4;
typedef __attribute__((ext_vector_type(4))) int i32x4;
typedef __attribute__((ext_vector_type(8))) __bf16 bf16x8;
typedef __attribute__((ext_vector_type(4))) float f32x4;

#define WN 6
#define SD 6
#define SR 12
#define HMAP 0
#define WMAP 0

#define SB() __builtin_amdgcn_sched_barrier(0)
#define CB() asm volatile("" ::: "memory")
#define VMW(n) __builtin_amdgcn_s_waitcnt(((n) & 15) | (7 << 4) | (15 << 8) | (((n) >> 4) << 14))
#define LGW0() __builtin_amdgcn_s_waitcnt(15 | (7 << 4) | (0 << 8) | (3 << 14))
constexpr int NHEAD = 512, SKV = 2048, DH = 128, RK = 64;
constexpr unsigned HEAD_BYTES = SKV * DH * 2;
constexpr unsigned PROJ_BYTES = RK * SKV * 2;
constexpr int NSTEP = 64;
constexpr int CS = 8 - WN;
constexpr int PS = CS + 2 > 7 ? 7 : CS + 2;
#define BX 1
constexpr int LAG = (SD - 2 - BX) < (SR - SD - 1 - BX) ? (SD - 2 - BX) : (SR - SD - 1 - BX);

constexpr int RING = (SR < 10 ? 10 : SR) * 8192;
constexpr int LDS_BYTES = RING + 4096 + 64 + 4096;
constexpr int vm_at(int k) {
  int n = WN - 1;
  for (int i = 1; i <= WN; ++i)
    if (((k - i) & 7) == PS) n += 2;
  return n;
}

template <int CTRL>
__device__ __forceinline__ float dpp(float x) {
  return __builtin_bit_cast(float, __builtin_amdgcn_mov_dpp(__builtin_bit_cast(int, x), CTRL, 0xf, 0xf, true));
}
constexpr int XOR1 = 0xB1, XOR2 = 0x4E, XOR7 = 0x141, XOR8 = 0x128, ROR4 = 0x124;

__device__ __forceinline__ float xrow_sum(float x) {
  auto s = __builtin_amdgcn_permlane16_swap(__float_as_uint(x), __float_as_uint(x), false, false);
  x = __uint_as_float(s[0]) + __uint_as_float(s[1]);
  auto t = __builtin_amdgcn_permlane32_swap(__float_as_uint(x), __float_as_uint(x), false, false);
  return __uint_as_float(t[0]) + __uint_as_float(t[1]);
}
__device__ __forceinline__ float xrow_max(float x) {
  auto s = __builtin_amdgcn_permlane16_swap(__float_as_uint(x), __float_as_uint(x), false, false);
  x = fmaxf(__uint_as_float(s[0]), __uint_as_float(s[1]));
  auto t = __builtin_amdgcn_permlane32_swap(__float_as_uint(x), __float_as_uint(x), false, false);
  return fmaxf(__uint_as_float(t[0]), __uint_as_float(t[1]));
}

struct Tile {
  u32x4 d[8];
};

template <int J0, int J1>
__device__ __forceinline__ void compute_cols(const Tile& t, const u32x4 (&p)[4], f32x4 (&acc)[4][8]) {
#pragma unroll
  for (int j = J0; j < J1; ++j) {
    u32x4 bf;
#pragma unroll
    for (int m = 0; m < 4; ++m)
      bf[m] = __builtin_amdgcn_perm(t.d[2 * m + 1][j >> 1], t.d[2 * m][j >> 1], (j & 1) ? 0x07060302u : 0x05040100u);
    const bf16x8 B = __builtin_bit_cast(bf16x8, bf);
#pragma unroll
    for (int b = 0; b < 4; ++b)
      acc[b][j] = __builtin_amdgcn_mfma_f32_16x16x32_bf16(__builtin_bit_cast(bf16x8, p[b]), B, acc[b][j], 0, 0, 0);
  }
}

struct Ctx {
  const char *kb, *vb, *e, *f;
  unsigned voff_d, voff_s, bx;
};
__device__ __forceinline__ u32x4 load_kv(const Ctx& x, int t, int i) {
  const char* base = t < 32 ? x.kb : x.vb;
  __amdgpu_buffer_rsrc_t r = __builtin_amdgcn_make_buffer_rsrc((void*)base, 0, t < NSTEP ? HEAD_BYTES : 0u, 0x00020000);
  return __builtin_amdgcn_raw_buffer_load_b128(r, x.voff_d + i * (DH * 2), ((t & 31) * (64 * DH * 2)) ^ x.bx, 2);
}
__device__ __forceinline__ u32x4 load_pj(const Ctx& x, int t, int i) {
  const char* base = t < 32 ? x.e : x.f;
  __amdgpu_buffer_rsrc_t r = __builtin_amdgcn_make_buffer_rsrc((void*)base, 0, t < NSTEP ? PROJ_BYTES : 0u, 0x00020000);
  return __builtin_amdgcn_raw_buffer_load_b128(r, x.voff_s, (t & 31) * 128 + i * (8 * SKV * 2), 0);
}

__global__ void __launch_bounds__(256, 2)
linformer_decode_kernel(const uint16_t* __restrict__ q, const uint16_t* __restrict__ k, const uint16_t* __restrict__ v,
                        const uint16_t* __restrict__ e, const uint16_t* __restrict__ f, uint16_t* __restrict__ out,
                        unsigned long long* stamps) {
  extern __shared__ __attribute__((aligned(16))) char lds[];
  float* lds_s = (float*)(lds + RING);
  float* lds_p = lds_s + 4 * RK;
  float* lds_o = lds_p + 4 * RK;
  int* prog = (int*)(lds + RING + 4096);

  const int wave = __builtin_amdgcn_readfirstlane(threadIdx.x >> 6);
  const int hsel = WMAP == 0 ? (wave >> 1) : (wave & 1), par = WMAP == 0 ? (wave & 1) : (wave >> 1);
  const int head = HMAP == 0 ? blockIdx.x * 2 + hsel : HMAP == 1 ? blockIdx.x + 256 * hsel : ((blockIdx.x & 7) | ((blockIdx.x >> 3) << 4)) + 8 * hsel;
  const int lane = threadIdx.x & 63;
  const int g = lane >> 4, c = lane & 15;
  Ctx x;
  x.kb = (const char*)k + (size_t)head * HEAD_BYTES;
  x.vb = (const char*)v + (size_t)head * HEAD_BYTES;
  x.e = (const char*)e;
  x.f = (const char*)f;
  x.voff_d = par * (32 * DH * 2) + g * (8 * DH * 2) + c * 16;
  x.bx = BX * hsel * (64 * DH * 2);
  const unsigned rx = BX * hsel * 8192;
  x.voff_s = (16 * wave + (lane >> 3)) * (SKV * 2) + (lane & 7) * 16;
  const unsigned wo0 = (16 * wave + (lane >> 3)) * 128 + (((lane & 7) ^ g) << 4);
  const unsigned wo1 = (16 * wave + 8 + (lane >> 3)) * 128 + (((lane & 7) ^ (4 + g)) << 4);
  const unsigned ro = c * 128 + (((4 * par + g) ^ (c >> 1)) << 4);

  Tile t0, t1;
  u32x4 pa[4], st[2];
#pragma unroll
  for (int i = 0; i < WN; ++i) t0.d[i] = load_kv(x, 0, i);
  SB();
  u32x4 qv = *reinterpret_cast<const u32x4*>(q + head * DH + c * 8);
  u32x4 s0[SD][2];
#pragma unroll
  for (int j = 0; j < SD; ++j) s0[j][0] = load_pj(x, j, 0), s0[j][1] = load_pj(x, j, 1);
  st[0] = load_pj(x, SD, 0), st[1] = load_pj(x, SD, 1);
  SB();
#define PSLOT(kk)                                                        \
  {                                                                      \
    VMW(kk < WN ? WN - 1 + 2 * SD + 3 : WN - 1);                         \
    if (kk < CS) t0.d[(WN + kk) & 7] = load_kv(x, 0, WN + kk);           \
    else t1.d[(kk - CS) & 7] = load_kv(x, 1, kk - CS);                   \
    SB();                                                                \
    if (kk == (WN < 7 ? WN : 7)) {                                       \
      asm volatile("" : "+v"(qv));                                       \
      _Pragma("unroll") for (int j = 0; j < SD; ++j) {                   \
        *reinterpret_cast<u32x4*>(lds + j * 8192 + wo0) = s0[j][0];      \
        *reinterpret_cast<u32x4*>(lds + j * 8192 + wo1) = s0[j][1];      \
      }                                                                  \
      prog[wave] = 0;                                                    \
      SB();                                                              \
    }                                                                    \
  }
  PSLOT(0) PSLOT(1) PSLOT(2) PSLOT(3) PSLOT(4) PSLOT(5) PSLOT(6) PSLOT(7)
#undef PSLOT
  f32x4 acc[4][8];
#pragma unroll
  for (int b = 0; b < 4; ++b)
#pragma unroll
    for (int j = 0; j < 8; ++j) acc[b][j] = f32x4{0.f, 0.f, 0.f, 0.f};
  CB();
  LGW0();
  __builtin_amdgcn_s_barrier();
  CB();
#pragma unroll
  for (int b = 0; b < 4; ++b) pa[b] = *reinterpret_cast<const u32x4*>(lds + rx + b * 2048 + ro);
  SB();

  unsigned wslot = (SD % SR) * 8192, rslot = 8192;
  f32x4 pr[4];
  const unsigned prog_a = RING + 4096 + 4 * (lane & 3), prog_w = RING + 4096 + 4 * wave;

#define TL_MARK(i)
  TL_MARK(126)
#define TR_STEP(HALF, CTRL, BIT)                                              \
  _Pragma("unroll") for (int h = 0; h < (HALF); ++h) {                        \
    const float lo_ = xs[h], hi_ = xs[h + (HALF)];                            \
    const float t1_ = lo_ + dpp<CTRL>(lo_), t2_ = hi_ + dpp<CTRL>(hi_);       \
    xs[h] = (c & (BIT)) ? t2_ : t1_;                                          \
  }
#define STEP(TC, TN, LAST) SLOT(TC, TN, LAST, 0) SLOT(TC, TN, LAST, 1) SLOT(TC, TN, LAST, 2) SLOT(TC, TN, LAST, 3) \
  SLOT(TC, TN, LAST, 4) SLOT(TC, TN, LAST, 5) SLOT(TC, TN, LAST, 6) SLOT(TC, TN, LAST, 7)
#define SLOT(TC, TN, LAST, kk)                                                                \
  {                                                                                           \
    if (kk <= CS) {                                                                           \
      if (kk == 0) compute_cols<0, 8 / (CS + 1) + (8 % (CS + 1) > 0)>(TC, pa, acc);          \
      if (kk == 1) compute_cols<8 / (CS + 1) + (8 % (CS + 1) > 0), CS == 1 ? 8 : CS == 2 ? 6 : 4>(TC, pa, acc); \
      if (kk == 2) compute_cols<CS == 2 ? 6 : 4, CS == 2 ? 8 : 6>(TC, pa, acc);              \
      if (kk == 3) compute_cols<6, 8>(TC, pa, acc);                                           \
      SB();                                                                                   \
    }                                                                                         \
    if (LAST == 0 || (LAST == 1 && kk < CS)) {                                                \
      VMW(vm_at(kk));                                                                         \
      if (kk < CS) TN.d[(WN + kk) & 7] = load_kv(x, t + 1, WN + kk);                          \
      else TC.d[(kk - CS) & 7] = load_kv(x, t + 2, kk - CS);                                  \
      SB();                                                                                   \
    }                                                                                         \
    if (kk == PS && LAST < 2) {                                                               \
      int pg;                                                                                 \
      TL_MARK(2 * t)                                                                          \
      asm volatile(                                                                           \
          "1:\n\tds_read_b32 %0, %1\n\ts_waitcnt lgkmcnt(0)\n\t"                              \
          "v_cmp_gt_i32 vcc, %2, %0\n\ts_nop 1\n\ts_cbranch_vccz 2f\n\t"                      \
          "s_sleep 1\n\ts_branch 1b\n2:"                                                      \
          : "=&v"(pg) : "v"(prog_a), "s"(t - LAG) : "memory", "vcc");                         \
      TL_MARK(2 * t + 1)                                                                      \
      if (LAST == 0) {                                                                        \
        *reinterpret_cast<u32x4*>(lds + wslot + wo0) = st[0];                                 \
        *reinterpret_cast<u32x4*>(lds + wslot + wo1) = st[1];                                 \
      }                                                                                       \
      _Pragma("unroll") for (int b = 0; b < 4; ++b)                                           \
        pa[b] = *reinterpret_cast<const u32x4*>(lds + (rslot ^ rx) + b * 2048 + ro);                 \
      asm volatile("ds_write_b32 %0, %1" :: "v"(prog_w), "v"(t + 1) : "memory");              \
      wslot = wslot == (SR - 1) * 8192 ? 0 : wslot + 8192;                                    \
      rslot = rslot == (SR - 1) * 8192 ? 0 : rslot + 8192;                                    \
      SB();                                                                                   \
      if (LAST == 0) st[0] = load_pj(x, t + SD + 1, 0), st[1] = load_pj(x, t + SD + 1, 1);    \
      SB();                                                                                   \
    }                                                                                         \
  }

#pragma unroll 1
  for (int tt = 0; tt < NSTEP - 2; tt += 2) {
    asm volatile("" : "+v"(x.voff_d));
    int t = tt;
    STEP(t0, t1, 0)
    t = tt + 1;
    STEP(t1, t0, 0)
    if (tt == 30) {
      float qf[8], xs[16];
#pragma unroll
      for (int j = 0; j < 4; ++j) {
        qf[2 * j] = __uint_as_float(qv[j] << 16);
        qf[2 * j + 1] = __uint_as_float(qv[j] & 0xffff0000u);
      }
#pragma unroll
      for (int b = 0; b < 4; ++b)
#pragma unroll
        for (int m = 0; m < 4; ++m) {
          float a = 0.f;
#pragma unroll
          for (int j = 0; j < 8; ++j) a = fmaf(qf[j], acc[b][j][m], a);
          xs[b * 4 + m] = a;
        }
      TR_STEP(8, XOR8, 8) TR_STEP(4, XOR7, 4) TR_STEP(2, XOR2, 2) TR_STEP(1, XOR1, 1)
      lds_s[wave * RK + 16 * (c >> 2) + 4 * g + (c & 3)] = xs[0];
#pragma unroll
      for (int b = 0; b < 4; ++b)
#pragma unroll
        for (int j = 0; j < 8; ++j) acc[b][j] = f32x4{0.f, 0.f, 0.f, 0.f};
      CB();
    }
  }
  {
    int t = NSTEP - 2;
    STEP(t0, t1, 1)
    const int w0 = WMAP == 0 ? (wave & 2) : (wave & 1), w1 = WMAP == 0 ? w0 + 1 : w0 + 2;
    float sc = (lds_s[w0 * RK + lane] + lds_s[w1 * RK + lane]) * 0.08838834764831845f;
    float mx = sc;
    mx = fmaxf(mx, dpp<XOR1>(mx));
    mx = fmaxf(mx, dpp<XOR2>(mx));
    mx = fmaxf(mx, dpp<ROR4>(mx));
    mx = fmaxf(mx, dpp<XOR8>(mx));
    mx = xrow_max(mx);
    const float pe = __expf(sc - mx);
    float sum = pe;
    sum += dpp<XOR1>(sum);
    sum += dpp<XOR2>(sum);
    sum += dpp<ROR4>(sum);
    sum += dpp<XOR8>(sum);
    sum = xrow_sum(sum);
    lds_p[wave * RK + lane] = pe / sum;
    CB();
#pragma unroll
    for (int b = 0; b < 4; ++b) pr[b] = *reinterpret_cast<const f32x4*>(lds_p + wave * RK + 16 * b + 4 * g);
    SB();
    t = NSTEP - 1;
    STEP(t1, t0, 2)
  }
  float o[8];
#pragma unroll
  for (int j = 0; j < 8; ++j) {
    float a = 0.f;
#pragma unroll
    for (int b = 0; b < 4; ++b)
#pragma unroll
      for (int m = 0; m < 4; ++m) a = fmaf(pr[b][m], acc[b][j][m], a);
    o[j] = xrow_sum(a);
  }
  if (g == 0) {
    *reinterpret_cast<f32x4*>(lds_o + wave * DH + 8 * c) = f32x4{o[0], o[1], o[2], o[3]};
    *reinterpret_cast<f32x4*>(lds_o + wave * DH + 8 * c + 4) = f32x4{o[4], o[5], o[6], o[7]};
  }
  __syncthreads();
  if (par == 0) {
    const int w1 = WMAP == 0 ? wave + 1 : wave + 2;
    const int d0 = 2 * lane;
    float r0 = lds_o[wave * DH + d0] + lds_o[w1 * DH + d0];
    float r1 = lds_o[wave * DH + d0 + 1] + lds_o[w1 * DH + d0 + 1];
    __hip_bfloat162 pk;
    pk.x = __float2bfloat16(r0);
    pk.y = __float2bfloat16(r1);
    *reinterpret_cast<__hip_bfloat162*>(out + head * DH + d0) = pk;
  }
#undef STEP
#undef SLOT
#undef TR_STEP
#undef TL_MARK
}

extern "C" void kernel_launch(void* const* d_in, const int* in_sizes, int n_in, void* d_out, int out_size, void* d_ws,
                              size_t ws_size, hipStream_t stream) {
  const uint16_t* q = (const uint16_t*)d_in[0];
  const uint16_t* k = (const uint16_t*)d_in[1];
  const uint16_t* v = (const uint16_t*)d_in[2];
  const uint16_t* e = (const uint16_t*)d_in[3];
  const uint16_t* f = (const uint16_t*)d_in[4];
  (void)hipFuncSetAttribute((const void*)linformer_decode_kernel, hipFuncAttributeMaxDynamicSharedMemorySize, LDS_BYTES);
  linformer_decode_kernel<<<NHEAD / 2, 256, LDS_BYTES, stream>>>(q, k, v, e, f, (uint16_t*)d_out, (unsigned long long*)d_ws);
}
```

```cpp
#include <hip/hip_runtime.h>
#include <hip/hip_bf16.h>
#include <stdint.h>

typedef __attribute__((ext_vector_type(4))) unsigned u32x4;
typedef __attribute__((ext_vector_type(4))) int i32x4;
typedef __attribute__((ext_vector_type(8))) __bf16 bf16x8;
typedef __attribute__((ext_vector_type(4))) float f32x4;

#define WN 6
#define SD 6
#define SR 12
#define HMAP 0
#define WMAP 0

#define SB() __builtin_amdgcn_sched_barrier(0)
#define CB() asm volatile("" ::: "memory")
#define VMW(n) __builtin_amdgcn_s_waitcnt(((n) & 15) | (7 << 4) | (15 << 8) | (((n) >> 4) << 14))
#define LGW0() __builtin_amdgcn_s_waitcnt(15 | (7 << 4) | (0 << 8) | (3 << 14))
constexpr int NHEAD = 512, SKV = 2048, DH = 128, RK = 64;
constexpr unsigned HEAD_BYTES = SKV * DH * 2;
constexpr unsigned PROJ_BYTES = RK * SKV * 2;
constexpr int NSTEP = 64;
constexpr int CS = 8 - WN;
constexpr int PS = CS + 2 > 7 ? 7 : CS + 2;
#define BX 1
constexpr int LAG = (SD - 2 - BX) < (SR - SD - 1 - BX) ? (SD - 2 - BX) : (SR - SD - 1 - BX);

constexpr int RING = (SR < 10 ? 10 : SR) * 8192;
constexpr int LDS_BYTES = RING + 4096 + 64;
constexpr int vm_at(int k) {
  int n = WN - 1;
  for (int i = 1; i <= WN; ++i)
    if (((k - i) & 7) == PS) n += 2;
  return n;
}

template <int CTRL>
__device__ __forceinline__ float dpp(float x) {
  return __builtin_bit_cast(float, __builtin_amdgcn_mov_dpp(__builtin_bit_cast(int, x), CTRL, 0xf, 0xf, true));
}
constexpr int XOR1 = 0xB1, XOR2 = 0x4E, XOR7 = 0x141, XOR8 = 0x128, ROR4 = 0x124;

__device__ __forceinline__ float xrow_sum(float x) {
  auto s = __builtin_amdgcn_permlane16_swap(__float_as_uint(x), __float_as_uint(x), false, false);
  x = __uint_as_float(s[0]) + __uint_as_float(s[1]);
  auto t = __builtin_amdgcn_permlane32_swap(__float_as_uint(x), __float_as_uint(x), false, false);
  return __uint_as_float(t[0]) + __uint_as_float(t[1]);
}
__device__ __forceinline__ float xrow_max(float x) {
  auto s = __builtin_amdgcn_permlane16_swap(__float_as_uint(x), __float_as_uint(x), false, false);
  x = fmaxf(__uint_as_float(s[0]), __uint_as_float(s[1]));
  auto t = __builtin_amdgcn_permlane32_swap(__float_as_uint(x), __float_as_uint(x), false, false);
  return fmaxf(__uint_as_float(t[0]), __uint_as_float(t[1]));
}

struct Tile {
  u32x4 d[8];
};

template <int J0, int J1>
__device__ __forceinline__ void compute_cols(const Tile& t, const u32x4 (&p)[4], f32x4 (&acc)[4][8]) {
#pragma unroll
  for (int j = J0; j < J1; ++j) {
    u32x4 bf;
#pragma unroll
    for (int m = 0; m < 4; ++m)
      bf[m] = __builtin_amdgcn_perm(t.d[2 * m + 1][j >> 1], t.d[2 * m][j >> 1], (j & 1) ? 0x07060302u : 0x05040100u);
    const bf16x8 B = __builtin_bit_cast(bf16x8, bf);
#pragma unroll
    for (int b = 0; b < 4; ++b)
      acc[b][j] = __builtin_amdgcn_mfma_f32_16x16x32_bf16(__builtin_bit_cast(bf16x8, p[b]), B, acc[b][j], 0, 0, 0);
  }
}

struct Ctx {
  const char *kb, *vb, *e, *f;
  unsigned voff_d, voff_s, bx;
};
__device__ __forceinline__ u32x4 load_kv(const Ctx& x, int t, int i) {
  const char* base = t < 32 ? x.kb : x.vb;
  __amdgpu_buffer_rsrc_t r = __builtin_amdgcn_make_buffer_rsrc((void*)base, 0, t < NSTEP ? HEAD_BYTES : 0u, 0x00020000);
  return __builtin_amdgcn_raw_buffer_load_b128(r, x.voff_d + i * (DH * 2), ((t & 31) * (64 * DH * 2)) ^ x.bx, 2);
}
__device__ __forceinline__ u32x4 load_pj(const Ctx& x, int t, int i) {
  const char* base = t < 32 ? x.e : x.f;
  __amdgpu_buffer_rsrc_t r = __builtin_amdgcn_make_buffer_rsrc((void*)base, 0, t < NSTEP ? PROJ_BYTES : 0u, 0x00020000);
  return __builtin_amdgcn_raw_buffer_load_b128(r, x.voff_s, (t & 31) * 128 + i * (8 * SKV * 2), 0);
}

__global__ void __launch_bounds__(256, 2)
linformer_decode_kernel(const uint16_t* __restrict__ q, const uint16_t* __restrict__ k, const uint16_t* __restrict__ v,
                        const uint16_t* __restrict__ e, const uint16_t* __restrict__ f, uint16_t* __restrict__ out) {
  extern __shared__ __attribute__((aligned(16))) char lds[];
  float* lds_s = (float*)(lds + RING);
  float* lds_p = lds_s + 4 * RK;
  float* lds_o = lds_p + 4 * RK;
  int* prog = (int*)(lds + RING + 4096);

  const int wave = __builtin_amdgcn_readfirstlane(threadIdx.x >> 6);
  const int hsel = WMAP == 0 ? (wave >> 1) : (wave & 1), par = WMAP == 0 ? (wave & 1) : (wave >> 1);
  const int head = HMAP == 0 ? blockIdx.x * 2 + hsel : HMAP == 1 ? blockIdx.x + 256 * hsel : ((blockIdx.x & 7) | ((blockIdx.x >> 3) << 4)) + 8 * hsel;
  const int lane = threadIdx.x & 63;
  const int g = lane >> 4, c = lane & 15;
  Ctx x;
  x.kb = (const char*)k + (size_t)head * HEAD_BYTES;
  x.vb = (const char*)v + (size_t)head * HEAD_BYTES;
  x.e = (const char*)e;
  x.f = (const char*)f;
  x.voff_d = par * (32 * DH * 2) + g * (8 * DH * 2) + c * 16;
  x.bx = BX * hsel * (64 * DH * 2);
  const unsigned rx = BX * hsel * 8192;
  x.voff_s = (16 * wave + (lane >> 3)) * (SKV * 2) + (lane & 7) * 16;
  const unsigned wo0 = (16 * wave + (lane >> 3)) * 128 + (((lane & 7) ^ g) << 4);
  const unsigned wo1 = (16 * wave + 8 + (lane >> 3)) * 128 + (((lane & 7) ^ (4 + g)) << 4);
  const unsigned ro = c * 128 + (((4 * par + g) ^ (c >> 1)) << 4);

  Tile t0, t1;
  u32x4 pa[4], st[2];
#pragma unroll
  for (int i = 0; i < WN; ++i) t0.d[i] = load_kv(x, 0, i);
  SB();
  u32x4 qv = *reinterpret_cast<const u32x4*>(q + head * DH + c * 8);
  u32x4 s0[SD][2];
#pragma unroll
  for (int j = 0; j < SD; ++j) s0[j][0] = load_pj(x, j, 0), s0[j][1] = load_pj(x, j, 1);
  st[0] = load_pj(x, SD, 0), st[1] = load_pj(x, SD, 1);
  SB();
#define PSLOT(kk)                                                        \
  {                                                                      \
    VMW(kk < WN ? WN - 1 + 2 * SD + 3 : WN - 1);                         \
    if (kk < CS) t0.d[(WN + kk) & 7] = load_kv(x, 0, WN + kk);           \
    else t1.d[(kk - CS) & 7] = load_kv(x, 1, kk - CS);                   \
    SB();                                                                \
    if (kk == (WN < 7 ? WN : 7)) {                                       \
      asm volatile("" : "+v"(qv));                                       \
      _Pragma("unroll") for (int j = 0; j < SD; ++j) {                   \
        *reinterpret_cast<u32x4*>(lds + j * 8192 + wo0) = s0[j][0];      \
        *reinterpret_cast<u32x4*>(lds + j * 8192 + wo1) = s0[j][1];      \
      }                                                                  \
      prog[wave] = 0;                                                    \
      SB();                                                              \
    }                                                                    \
  }
  PSLOT(0) PSLOT(1) PSLOT(2) PSLOT(3) PSLOT(4) PSLOT(5) PSLOT(6) PSLOT(7)
#undef PSLOT
  f32x4 acc[4][8];
#pragma unroll
  for (int b = 0; b < 4; ++b)
#pragma unroll
    for (int j = 0; j < 8; ++j) acc[b][j] = f32x4{0.f, 0.f, 0.f, 0.f};
  CB();
  LGW0();
  __builtin_amdgcn_s_barrier();
  CB();
#pragma unroll
  for (int b = 0; b < 4; ++b) pa[b] = *reinterpret_cast<const u32x4*>(lds + rx + b * 2048 + ro);
  SB();

  unsigned wslot = (SD % SR) * 8192, rslot = 8192;
  f32x4 pr[4];
  const unsigned prog_a = RING + 4096 + 4 * (lane & 3), prog_w = RING + 4096 + 4 * wave;

#define TR_STEP(HALF, CTRL, BIT)                                              \
  _Pragma("unroll") for (int h = 0; h < (HALF); ++h) {                        \
    const float lo_ = xs[h], hi_ = xs[h + (HALF)];                            \
    const float t1_ = lo_ + dpp<CTRL>(lo_), t2_ = hi_ + dpp<CTRL>(hi_);       \
    xs[h] = (c & (BIT)) ? t2_ : t1_;                                          \
  }
#define STEP(TC, TN, LAST) SLOT(TC, TN, LAST, 0) SLOT(TC, TN, LAST, 1) SLOT(TC, TN, LAST, 2) SLOT(TC, TN, LAST, 3) \
  SLOT(TC, TN, LAST, 4) SLOT(TC, TN, LAST, 5) SLOT(TC, TN, LAST, 6) SLOT(TC, TN, LAST, 7)
#define SLOT(TC, TN, LAST, kk)                                                                \
  {                                                                                           \
    if (kk <= CS) {                                                                           \
      if (kk == 0) compute_cols<0, 8 / (CS + 1) + (8 % (CS + 1) > 0)>(TC, pa, acc);          \
      if (kk == 1) compute_cols<8 / (CS + 1) + (8 % (CS + 1) > 0), CS == 1 ? 8 : CS == 2 ? 6 : 4>(TC, pa, acc); \
      if (kk == 2) compute_cols<CS == 2 ? 6 : 4, CS == 2 ? 8 : 6>(TC, pa, acc);              \
      if (kk == 3) compute_cols<6, 8>(TC, pa, acc);                                           \
      SB();                                                                                   \
    }                                                                                         \
    if (LAST == 0 || (LAST == 1 && kk < CS)) {                                                \
      VMW(vm_at(kk));                                                                         \
      if (kk < CS) TN.d[(WN + kk) & 7] = load_kv(x, t + 1, WN + kk);                          \
      else TC.d[(kk - CS) & 7] = load_kv(x, t + 2, kk - CS);                                  \
      SB();                                                                                   \
    }                                                                                         \
    if (kk == PS && LAST < 2) {                                                               \
      int pg;                                                                                 \
      asm volatile(                                                                           \
          "1:\n\tds_read_b32 %0, %1\n\ts_waitcnt lgkmcnt(0)\n\t"                              \
          "v_cmp_gt_i32 vcc, %2, %0\n\ts_nop 1\n\ts_cbranch_vccz 2f\n\t"                      \
          "s_sleep 1\n\ts_branch 1b\n2:"                                                      \
          : "=&v"(pg) : "v"(prog_a), "s"(t - LAG) : "memory", "vcc");                         \
      if (LAST == 0) {                                                                        \
        *reinterpret_cast<u32x4*>(lds + wslot + wo0) = st[0];                                 \
        *reinterpret_cast<u32x4*>(lds + wslot + wo1) = st[1];                                 \
      }                                                                                       \
      _Pragma("unroll") for (int b = 0; b < 4; ++b)                                           \
        pa[b] = *reinterpret_cast<const u32x4*>(lds + (rslot ^ rx) + b * 2048 + ro);                 \
      asm volatile("ds_write_b32 %0, %1" :: "v"(prog_w), "v"(t + 1) : "memory");              \
      wslot = wslot == (SR - 1) * 8192 ? 0 : wslot + 8192;                                    \
      rslot = rslot == (SR - 1) * 8192 ? 0 : rslot + 8192;                                    \
      SB();                                                                                   \
      if (LAST == 0) st[0] = load_pj(x, t + SD + 1, 0), st[1] = load_pj(x, t + SD + 1, 1);    \
      SB();                                                                                   \
    }                                                                                         \
  }

#pragma unroll 1
  for (int tt = 0; tt < NSTEP - 2; tt += 2) {
    asm volatile("" : "+v"(x.voff_d));
    int t = tt;
    STEP(t0, t1, 0)
    t = tt + 1;
    STEP(t1, t0, 0)
    if (tt == 30) {
      float qf[8], xs[16];
#pragma unroll
      for (int j = 0; j < 4; ++j) {
        qf[2 * j] = __uint_as_float(qv[j] << 16);
        qf[2 * j + 1] = __uint_as_float(qv[j] & 0xffff0000u);
      }
#pragma unroll
      for (int b = 0; b < 4; ++b)
#pragma unroll
        for (int m = 0; m < 4; ++m) {
          float a = 0.f;
#pragma unroll
          for (int j = 0; j < 8; ++j) a = fmaf(qf[j], acc[b][j][m], a);
          xs[b * 4 + m] = a;
        }
      TR_STEP(8, XOR8, 8) TR_STEP(4, XOR7, 4) TR_STEP(2, XOR2, 2) TR_STEP(1, XOR1, 1)
      lds_s[wave * RK + 16 * (c >> 2) + 4 * g + (c & 3)] = xs[0];
#pragma unroll
      for (int b = 0; b < 4; ++b)
#pragma unroll
        for (int j = 0; j < 8; ++j) acc[b][j] = f32x4{0.f, 0.f, 0.f, 0.f};
      CB();
    }
  }
  {
    int t = NSTEP - 2;
    STEP(t0, t1, 1)
    const int w0 = WMAP == 0 ? (wave & 2) : (wave & 1), w1 = WMAP == 0 ? w0 + 1 : w0 + 2;
    float sc = (lds_s[w0 * RK + lane] + lds_s[w1 * RK + lane]) * 0.08838834764831845f;
    float mx = sc;
    mx = fmaxf(mx, dpp<XOR1>(mx));
    mx = fmaxf(mx, dpp<XOR2>(mx));
    mx = fmaxf(mx, dpp<ROR4>(mx));
    mx = fmaxf(mx, dpp<XOR8>(mx));
    mx = xrow_max(mx);
    const float pe = __expf(sc - mx);
    float sum = pe;
    sum += dpp<XOR1>(sum);
    sum += dpp<XOR2>(sum);
    sum += dpp<ROR4>(sum);
    sum += dpp<XOR8>(sum);
    sum = xrow_sum(sum);
    lds_p[wave * RK + lane] = pe / sum;
    CB();
#pragma unroll
    for (int b = 0; b < 4; ++b) pr[b] = *reinterpret_cast<const f32x4*>(lds_p + wave * RK + 16 * b + 4 * g);
    SB();
    t = NSTEP - 1;
    STEP(t1, t0, 2)
  }
  float o[8];
#pragma unroll
  for (int j = 0; j < 8; ++j) {
    float a = 0.f;
#pragma unroll
    for (int b = 0; b < 4; ++b)
#pragma unroll
      for (int m = 0; m < 4; ++m) a = fmaf(pr[b][m], acc[b][j][m], a);
    o[j] = xrow_sum(a);
  }
  if (g == 0) {
    *reinterpret_cast<f32x4*>(lds_o + wave * DH + 8 * c) = f32x4{o[0], o[1], o[2], o[3]};
    *reinterpret_cast<f32x4*>(lds_o + wave * DH + 8 * c + 4) = f32x4{o[4], o[5], o[6], o[7]};
  }
  __syncthreads();
  if (par == 0) {
    const int w1 = WMAP == 0 ? wave + 1 : wave + 2;
    const int d0 = 2 * lane;
    float r0 = lds_o[wave * DH + d0] + lds_o[w1 * DH + d0];
    float r1 = lds_o[wave * DH + d0 + 1] + lds_o[w1 * DH + d0 + 1];
    __hip_bfloat162 pk;
    pk.x = __float2bfloat16(r0);
    pk.y = __float2bfloat16(r1);
    *reinterpret_cast<__hip_bfloat162*>(out + head * DH + d0) = pk;
  }
#undef STEP
#undef SLOT
#undef TR_STEP
}

extern "C" void kernel_launch(void* const* d_in, const int* in_sizes, int n_in, void* d_out, int out_size, void* d_ws,
                              size_t ws_size, hipStream_t stream) {
  const uint16_t* q = (const uint16_t*)d_in[0];
  const uint16_t* k = (const uint16_t*)d_in[1];
  const uint16_t* v = (const uint16_t*)d_in[2];
  const uint16_t* e = (const uint16_t*)d_in[3];
  const uint16_t* f = (const uint16_t*)d_in[4];
  (void)hipFuncSetAttribute((const void*)linformer_decode_kernel, hipFuncAttributeMaxDynamicSharedMemorySize, LDS_BYTES);
  linformer_decode_kernel<<<NHEAD / 2, 256, LDS_BYTES, stream>>>(q, k, v, e, f, (uint16_t*)d_out);
}
```

```cpp
#define TOUCH 1
#include <hip/hip_runtime.h>
#include <hip/hip_bf16.h>
#include <stdint.h>

typedef __attribute__((ext_vector_type(4))) unsigned u32x4;
typedef __attribute__((ext_vector_type(4))) int i32x4;
typedef __attribute__((ext_vector_type(8))) __bf16 bf16x8;
typedef __attribute__((ext_vector_type(4))) float f32x4;

#define WN 6
#define SD 6
#define SR 12
#define HMAP 0
#define WMAP 0

#define SB() __builtin_amdgcn_sched_barrier(0)
#define CB() asm volatile("" ::: "memory")
#define VMW(n) __builtin_amdgcn_s_waitcnt(((n) & 15) | (7 << 4) | (15 << 8) | (((n) >> 4) << 14))
#define LGW0() __builtin_amdgcn_s_waitcnt(15 | (7 << 4) | (0 << 8) | (3 << 14))
constexpr int NHEAD = 512, SKV = 2048, DH = 128, RK = 64;
constexpr unsigned HEAD_BYTES = SKV * DH * 2;
constexpr unsigned PROJ_BYTES = RK * SKV * 2;
constexpr int NSTEP = 64;
constexpr int CS = 8 - WN;
constexpr int PS = CS + 2 > 7 ? 7 : CS + 2;
#define BX 1
constexpr int LAG = (SD - 2 - BX) < (SR - SD - 1 - BX) ? (SD - 2 - BX) : (SR - SD - 1 - BX);

constexpr int RING = (SR < 10 ? 10 : SR) * 8192;
constexpr int LDS_BYTES = RING + 4096 + 64 + 4096;
constexpr int vm_at(int k) {
  int n = WN - 1;
  for (int i = 1; i <= WN; ++i)
    if (((k - i) & 7) == PS) n += 2;
  return n;
}

template <int CTRL>
__device__ __forceinline__ float dpp(float x) {
  return __builtin_bit_cast(float, __builtin_amdgcn_mov_dpp(__builtin_bit_cast(int, x), CTRL, 0xf, 0xf, true));
}
constexpr int XOR1 = 0xB1, XOR2 = 0x4E, XOR7 = 0x141, XOR8 = 0x128, ROR4 = 0x124;

__device__ __forceinline__ float xrow_sum(float x) {
  auto s = __builtin_amdgcn_permlane16_swap(__float_as_uint(x), __float_as_uint(x), false, false);
  x = __uint_as_float(s[0]) + __uint_as_float(s[1]);
  auto t = __builtin_amdgcn_permlane32_swap(__float_as_uint(x), __float_as_uint(x), false, false);
  return __uint_as_float(t[0]) + __uint_as_float(t[1]);
}
__device__ __forceinline__ float xrow_max(float x) {
  auto s = __builtin_amdgcn_permlane16_swap(__float_as_uint(x), __float_as_uint(x), false, false);
  x = fmaxf(__uint_as_float(s[0]), __uint_as_float(s[1]));
  auto t = __builtin_amdgcn_permlane32_swap(__float_as_uint(x), __float_as_uint(x), false, false);
  return fmaxf(__uint_as_float(t[0]), __uint_as_float(t[1]));
}

struct Tile {
  u32x4 d[8];
};

template <int J0, int J1>
__device__ __forceinline__ void compute_cols(const Tile& t, const u32x4 (&p)[4], f32x4 (&acc)[4][8]) {
#pragma unroll
  for (int j = J0; j < J1; ++j) {
    u32x4 bf;
#pragma unroll
    for (int m = 0; m < 4; ++m)
      bf[m] = __builtin_amdgcn_perm(t.d[2 * m + 1][j >> 1], t.d[2 * m][j >> 1], (j & 1) ? 0x07060302u : 0x05040100u);
    const bf16x8 B = __builtin_bit_cast(bf16x8, bf);
#pragma unroll
    for (int b = 0; b < 4; ++b)
      acc[b][j] = __builtin_amdgcn_mfma_f32_16x16x32_bf16(__builtin_bit_cast(bf16x8, p[b]), B, acc[b][j], 0, 0, 0);
  }
}

struct Ctx {
  const char *kb, *vb, *e, *f;
  unsigned voff_d, voff_s, bx;
};
__device__ __forceinline__ u32x4 load_kv(const Ctx& x, int t, int i) {
  const char* base = t < 32 ? x.kb : x.vb;
  __amdgpu_buffer_rsrc_t r = __builtin_amdgcn_make_buffer_rsrc((void*)base, 0, t < NSTEP ? HEAD_BYTES : 0u, 0x00020000);
  return __builtin_amdgcn_raw_buffer_load_b128(r, x.voff_d + i * (DH * 2), ((t & 31) * (64 * DH * 2)) ^ x.bx, 2);
}
__device__ __forceinline__ u32x4 load_pj(const Ctx& x, int t, int i) {
  const char* base = t < 32 ? x.e : x.f;
  __amdgpu_buffer_rsrc_t r = __builtin_amdgcn_make_buffer_rsrc((void*)base, 0, t < NSTEP ? PROJ_BYTES : 0u, 0x00020000);
  return __builtin_amdgcn_raw_buffer_load_b128(r, x.voff_s, (t & 31) * 128 + i * (8 * SKV * 2), 0);
}

__global__ void __launch_bounds__(256, 2)
linformer_decode_kernel(const uint16_t* __restrict__ q, const uint16_t* __restrict__ k, const uint16_t* __restrict__ v,
                        const uint16_t* __restrict__ e, const uint16_t* __restrict__ f, uint16_t* __restrict__ out,
                        unsigned long long* stamps) {
  extern __shared__ __attribute__((aligned(16))) char lds[];
  float* lds_s = (float*)(lds + RING);
  float* lds_p = lds_s + 4 * RK;
  float* lds_o = lds_p + 4 * RK;
  int* prog = (int*)(lds + RING + 4096);

  const int wave = __builtin_amdgcn_readfirstlane(threadIdx.x >> 6);
  const int hsel = WMAP == 0 ? (wave >> 1) : (wave & 1), par = WMAP == 0 ? (wave & 1) : (wave >> 1);
  const int head = HMAP == 0 ? blockIdx.x * 2 + hsel : HMAP == 1 ? blockIdx.x + 256 * hsel : ((blockIdx.x & 7) | ((blockIdx.x >> 3) << 4)) + 8 * hsel;
  const int lane = threadIdx.x & 63;
  const int g = lane >> 4, c = lane & 15;
  Ctx x;
  x.kb = (const char*)k + (size_t)head * HEAD_BYTES;
  x.vb = (const char*)v + (size_t)head * HEAD_BYTES;
  x.e = (const char*)e;
  x.f = (const char*)f;
  x.voff_d = par * (32 * DH * 2) + g * (8 * DH * 2) + c * 16;
  x.bx = BX * hsel * (64 * DH * 2);
  const unsigned rx = BX * hsel * 8192;
  x.voff_s = (16 * wave + (lane >> 3)) * (SKV * 2) + (lane & 7) * 16;
  const unsigned wo0 = (16 * wave + (lane >> 3)) * 128 + (((lane & 7) ^ g) << 4);
  const unsigned wo1 = (16 * wave + 8 + (lane >> 3)) * 128 + (((lane & 7) ^ (4 + g)) << 4);
  const unsigned ro = c * 128 + (((4 * par + g) ^ (c >> 1)) << 4);

  Tile t0, t1;
  u32x4 pa[4], st[2];
#pragma unroll
  for (int i = 0; i < WN; ++i) t0.d[i] = load_kv(x, 0, i);
  SB();
  u32x4 qv = *reinterpret_cast<const u32x4*>(q + head * DH + c * 8);
  u32x4 s0[SD][2];
#pragma unroll
  for (int j = 0; j < SD; ++j) s0[j][0] = load_pj(x, j, 0), s0[j][1] = load_pj(x, j, 1);
  st[0] = load_pj(x, SD, 0), st[1] = load_pj(x, SD, 1);
  SB();
  unsigned tch = 0;
  if (TOUCH) {
    const char* tp = (const char*)q + head * DH * 2;
    if ((TOUCH & 8) && lane < 8) tp = x.kb + lane * 65536;
    if ((TOUCH & 2) && lane >= 8 && lane < 16) tp = x.vb + (lane - 8) * 65536;
    if ((TOUCH & 4) && lane >= 16 && lane < 20) tp = x.f + (lane - 16) * 65536;
    if ((TOUCH & 1) && lane >= 20 && lane < 24) tp = (const char*)out + head * DH * 2;
    tch = __builtin_nontemporal_load((const unsigned*)tp);
  }
  SB();
#define PSLOT(kk)                                                        \
  {                                                                      \
    VMW(kk < WN ? WN - 1 + 2 * SD + 3 + (TOUCH ? 1 : 0) : WN - 1);                         \
    if (kk < CS) t0.d[(WN + kk) & 7] = load_kv(x, 0, WN + kk);           \
    else t1.d[(kk - CS) & 7] = load_kv(x, 1, kk - CS);                   \
    SB();                                                                \
    if (kk == (WN < 7 ? WN : 7)) {                                       \
      asm volatile("" : "+v"(qv)); asm volatile("" :: "v"(tch));                                       \
      _Pragma("unroll") for (int j = 0; j < SD; ++j) {                   \
        *reinterpret_cast<u32x4*>(lds + j * 8192 + wo0) = s0[j][0];      \
        *reinterpret_cast<u32x4*>(lds + j * 8192 + wo1) = s0[j][1];      \
      }                                                                  \
      prog[wave] = 0;                                                    \
      SB();                                                              \
    }                                                                    \
  }
  PSLOT(0) PSLOT(1) PSLOT(2) PSLOT(3) PSLOT(4) PSLOT(5) PSLOT(6) PSLOT(7)
#undef PSLOT
  f32x4 acc[4][8];
#pragma unroll
  for (int b = 0; b < 4; ++b)
#pragma unroll
    for (int j = 0; j < 8; ++j) acc[b][j] = f32x4{0.f, 0.f, 0.f, 0.f};
  CB();
  LGW0();
  __builtin_amdgcn_s_barrier();
  CB();
#pragma unroll
  for (int b = 0; b < 4; ++b) pa[b] = *reinterpret_cast<const u32x4*>(lds + rx + b * 2048 + ro);
  SB();

  unsigned wslot = (SD % SR) * 8192, rslot = 8192;
  f32x4 pr[4];
  const unsigned prog_a = RING + 4096 + 4 * (lane & 3), prog_w = RING + 4096 + 4 * wave;

#define TL_MARK(i)
  TL_MARK(126)
#define TR_STEP(HALF, CTRL, BIT)                                              \
  _Pragma("unroll") for (int h = 0; h < (HALF); ++h) {                        \
    const float lo_ = xs[h], hi_ = xs[h + (HALF)];                            \
    const float t1_ = lo_ + dpp<CTRL>(lo_), t2_ = hi_ + dpp<CTRL>(hi_);       \
    xs[h] = (c & (BIT)) ? t2_ : t1_;                                          \
  }
#define STEP(TC, TN, LAST) SLOT(TC, TN, LAST, 0) SLOT(TC, TN, LAST, 1) SLOT(TC, TN, LAST, 2) SLOT(TC, TN, LAST, 3) \
  SLOT(TC, TN, LAST, 4) SLOT(TC, TN, LAST, 5) SLOT(TC, TN, LAST, 6) SLOT(TC, TN, LAST, 7)
#define SLOT(TC, TN, LAST, kk)                                                                \
  {                                                                                           \
    if (kk <= CS) {                                                                           \
      if (kk == 0) compute_cols<0, 8 / (CS + 1) + (8 % (CS + 1) > 0)>(TC, pa, acc);          \
      if (kk == 1) compute_cols<8 / (CS + 1) + (8 % (CS + 1) > 0), CS == 1 ? 8 : CS == 2 ? 6 : 4>(TC, pa, acc); \
      if (kk == 2) compute_cols<CS == 2 ? 6 : 4, CS == 2 ? 8 : 6>(TC, pa, acc);              \
      if (kk == 3) compute_cols<6, 8>(TC, pa, acc);                                           \
      SB();                                                                                   \
    }                                                                                         \
    if (LAST == 0 || (LAST == 1 && kk < CS)) {                                                \
      VMW(vm_at(kk));                                                                         \
      if (kk < CS) TN.d[(WN + kk) & 7] = load_kv(x, t + 1, WN + kk);                          \
      else TC.d[(kk - CS) & 7] = load_kv(x, t + 2, kk - CS);                                  \
      SB();                                                                                   \
    }                                                                                         \
    if (kk == PS && LAST < 2) {                                                               \
      int pg;                                                                                 \
      TL_MARK(2 * t)                                                                          \
      asm volatile(                                                                           \
          "1:\n\tds_read_b32 %0, %1\n\ts_waitcnt lgkmcnt(0)\n\t"                              \
          "v_cmp_gt_i32 vcc, %2, %0\n\ts_nop 1\n\ts_cbranch_vccz 2f\n\t"                      \
          "s_sleep 1\n\ts_branch 1b\n2:"                                                      \
          : "=&v"(pg) : "v"(prog_a), "s"(t - LAG) : "memory", "vcc");                         \
      TL_MARK(2 * t + 1)                                                                      \
      if (LAST == 0) {                                                                        \
        *reinterpret_cast<u32x4*>(lds + wslot + wo0) = st[0];                                 \
        *reinterpret_cast<u32x4*>(lds + wslot + wo1) = st[1];                                 \
      }                                                                                       \
      _Pragma("unroll") for (int b = 0; b < 4; ++b)                                           \
        pa[b] = *reinterpret_cast<const u32x4*>(lds + (rslot ^ rx) + b * 2048 + ro);                 \
      asm volatile("ds_write_b32 %0, %1" :: "v"(prog_w), "v"(t + 1) : "memory");              \
      wslot = wslot == (SR - 1) * 8192 ? 0 : wslot + 8192;                                    \
      rslot = rslot == (SR - 1) * 8192 ? 0 : rslot + 8192;                                    \
      SB();                                                                                   \
      if (LAST == 0) st[0] = load_pj(x, t + SD + 1, 0), st[1] = load_pj(x, t + SD + 1, 1);    \
      SB();                                                                                   \
    }                                                                                         \
  }

#pragma unroll 1
  for (int tt = 0; tt < NSTEP - 2; tt += 2) {
    asm volatile("" : "+v"(x.voff_d));
    int t = tt;
    STEP(t0, t1, 0)
    t = tt + 1;
    STEP(t1, t0, 0)
    if (tt == 30) {
      float qf[8], xs[16];
#pragma unroll
      for (int j = 0; j < 4; ++j) {
        qf[2 * j] = __uint_as_float(qv[j] << 16);
        qf[2 * j + 1] = __uint_as_float(qv[j] & 0xffff0000u);
      }
#pragma unroll
      for (int b = 0; b < 4; ++b)
#pragma unroll
        for (int m = 0; m < 4; ++m) {
          float a = 0.f;
#pragma unroll
          for (int j = 0; j < 8; ++j) a = fmaf(qf[j], acc[b][j][m], a);
          xs[b * 4 + m] = a;
        }
      TR_STEP(8, XOR8, 8) TR_STEP(4, XOR7, 4) TR_STEP(2, XOR2, 2) TR_STEP(1, XOR1, 1)
      lds_s[wave * RK + 16 * (c >> 2) + 4 * g + (c & 3)] = xs[0];
#pragma unroll
      for (int b = 0; b < 4; ++b)
#pragma unroll
        for (int j = 0; j < 8; ++j) acc[b][j] = f32x4{0.f, 0.f, 0.f, 0.f};
      CB();
    }
  }
  {
    int t = NSTEP - 2;
    STEP(t0, t1, 1)
    const int w0 = WMAP == 0 ? (wave & 2) : (wave & 1), w1 = WMAP == 0 ? w0 + 1 : w0 + 2;
    float sc = (lds_s[w0 * RK + lane] + lds_s[w1 * RK + lane]) * 0.08838834764831845f;
    float mx = sc;
    mx = fmaxf(mx, dpp<XOR1>(mx));
    mx = fmaxf(mx, dpp<XOR2>(mx));
    mx = fmaxf(mx, dpp<ROR4>(mx));
    mx = fmaxf(mx, dpp<XOR8>(mx));
    mx = xrow_max(mx);
    const float pe = __expf(sc - mx);
    float sum = pe;
    sum += dpp<XOR1>(sum);
    sum += dpp<XOR2>(sum);
    sum += dpp<ROR4>(sum);
    sum += dpp<XOR8>(sum);
    sum = xrow_sum(sum);
    lds_p[wave * RK + lane] = pe / sum;
    CB();
#pragma unroll
    for (int b = 0; b < 4; ++b) pr[b] = *reinterpret_cast<const f32x4*>(lds_p + wave * RK + 16 * b + 4 * g);
    SB();
    t = NSTEP - 1;
    STEP(t1, t0, 2)
  }
  float o[8];
#pragma unroll
  for (int j = 0; j < 8; ++j) {
    float a = 0.f;
#pragma unroll
    for (int b = 0; b < 4; ++b)
#pragma unroll
      for (int m = 0; m < 4; ++m) a = fmaf(pr[b][m], acc[b][j][m], a);
    o[j] = xrow_sum(a);
  }
  if (g == 0) {
    *reinterpret_cast<f32x4*>(lds_o + wave * DH + 8 * c) = f32x4{o[0], o[1], o[2], o[3]};
    *reinterpret_cast<f32x4*>(lds_o + wave * DH + 8 * c + 4) = f32x4{o[4], o[5], o[6], o[7]};
  }
  __syncthreads();
  if (par == 0) {
    const int w1 = WMAP == 0 ? wave + 1 : wave + 2;
    const int d0 = 2 * lane;
    float r0 = lds_o[wave * DH + d0] + lds_o[w1 * DH + d0];
    float r1 = lds_o[wave * DH + d0 + 1] + lds_o[w1 * DH + d0 + 1];
    __hip_bfloat162 pk;
    pk.x = __float2bfloat16(r0);
    pk.y = __float2bfloat16(r1);
    *reinterpret_cast<__hip_bfloat162*>(out + head * DH + d0) = pk;
  }
#undef STEP
#undef SLOT
#undef TR_STEP
#undef TL_MARK
}

extern "C" void kernel_launch(void* const* d_in, const int* in_sizes, int n_in, void* d_out, int out_size, void* d_ws,
                              size_t ws_size, hipStream_t stream) {
  const uint16_t* q = (const uint16_t*)d_in[0];
  const uint16_t* k = (const uint16_t*)d_in[1];
  const uint16_t* v = (const uint16_t*)d_in[2];
  const uint16_t* e = (const uint16_t*)d_in[3];
  const uint16_t* f = (const uint16_t*)d_in[4];
  (void)hipFuncSetAttribute((const void*)linformer_decode_kernel, hipFuncAttributeMaxDynamicSharedMemorySize, LDS_BYTES);
  linformer_decode_kernel<<<NHEAD / 2, 256, LDS_BYTES, stream>>>(q, k, v, e, f, (uint16_t*)d_out, (unsigned long long*)d_ws);
}
```

```cpp
#include <hip/hip_runtime.h>
#include <hip/hip_bf16.h>
#include <stdint.h>

typedef __attribute__((ext_vector_type(4))) unsigned u32x4;
typedef __attribute__((ext_vector_type(4))) int i32x4;
typedef __attribute__((ext_vector_type(8))) __bf16 bf16x8;
typedef __attribute__((ext_vector_type(4))) float f32x4;

#define WN 6
#define SD 6
#define SR 12
#define HMAP 0
#define WMAP 0

#define SB() __builtin_amdgcn_sched_barrier(0)
#define CB() asm volatile("" ::: "memory")
#define VMW(n) __builtin_amdgcn_s_waitcnt(((n) & 15) | (7 << 4) | (15 << 8) | (((n) >> 4) << 14))
#define LGW0() __builtin_amdgcn_s_waitcnt(15 | (7 << 4) | (0 << 8) | (3 << 14))
constexpr int NHEAD = 512, SKV = 2048, DH = 128, RK = 64;
constexpr unsigned HEAD_BYTES = SKV * DH * 2;
constexpr unsigned PROJ_BYTES = RK * SKV * 2;
constexpr int NSTEP = 64;
constexpr int CS = 8 - WN;
constexpr int PS = CS + 2 > 7 ? 7 : CS + 2;
#define BX 1
constexpr int LAG = (SD - 2 - BX) < (SR - SD - 1 - BX) ? (SD - 2 - BX) : (SR - SD - 1 - BX);

constexpr int RING = (SR < 10 ? 10 : SR) * 8192;
constexpr int LDS_BYTES = RING + 4096 + 64;
constexpr int vm_at(int k) {
  int n = WN - 1;
  for (int i = 1; i <= WN; ++i)
    if (((k - i) & 7) == PS) n += 2;
  return n;
}

template <int CTRL>
__device__ __forceinline__ float dpp(float x) {
  return __builtin_bit_cast(float, __builtin_amdgcn_mov_dpp(__builtin_bit_cast(int, x), CTRL, 0xf, 0xf, true));
}
constexpr int XOR1 = 0xB1, XOR2 = 0x4E, XOR7 = 0x141, XOR8 = 0x128, ROR4 = 0x124;

__device__ __forceinline__ float xrow_sum(float x) {
  auto s = __builtin_amdgcn_permlane16_swap(__float_as_uint(x), __float_as_uint(x), false, false);
  x = __uint_as_float(s[0]) + __uint_as_float(s[1]);
  auto t = __builtin_amdgcn_permlane32_swap(__float_as_uint(x), __float_as_uint(x), false, false);
  return __uint_as_float(t[0]) + __uint_as_float(t[1]);
}
__device__ __forceinline__ float xrow_max(float x) {
  auto s = __builtin_amdgcn_permlane16_swap(__float_as_uint(x), __float_as_uint(x), false, false);
  x = fmaxf(__uint_as_float(s[0]), __uint_as_float(s[1]));
  auto t = __builtin_amdgcn_permlane32_swap(__float_as_uint(x), __float_as_uint(x), false, false);
  return fmaxf(__uint_as_float(t[0]), __uint_as_float(t[1]));
}

struct Tile {
  u32x4 d[8];
};

template <int J0, int J1>
__device__ __forceinline__ void compute_cols(const Tile& t, const u32x4 (&p)[4], f32x4 (&acc)[4][8]) {
#pragma unroll
  for (int j = J0; j < J1; ++j) {
    u32x4 bf;
#pragma unroll
    for (int m = 0; m < 4; ++m)
      bf[m] = __builtin_amdgcn_perm(t.d[2 * m + 1][j >> 1], t.d[2 * m][j >> 1], (j & 1) ? 0x07060302u : 0x05040100u);
    const bf16x8 B = __builtin_bit_cast(bf16x8, bf);
#pragma unroll
    for (int b = 0; b < 4; ++b)
      acc[b][j] = __builtin_amdgcn_mfma_f32_16x16x32_bf16(__builtin_bit_cast(bf16x8, p[b]), B, acc[b][j], 0, 0, 0);
  }
}

struct Ctx {
  const char *kb, *vb, *e, *f;
  unsigned voff_d, voff_s, bx;
};
__device__ __forceinline__ u32x4 load_kv(const Ctx& x, int t, int i) {
  const char* base = t < 32 ? x.kb : x.vb;
  __amdgpu_buffer_rsrc_t r = __builtin_amdgcn_make_buffer_rsrc((void*)base, 0, t < NSTEP ? HEAD_BYTES : 0u, 0x00020000);
  return __builtin_amdgcn_raw_buffer_load_b128(r, x.voff_d + i * (DH * 2), ((t & 31) * (64 * DH * 2)) ^ x.bx, 2);
}
__device__ __forceinline__ u32x4 load_pj(const Ctx& x, int t, int i) {
  const char* base = t < 32 ? x.e : x.f;
  __amdgpu_buffer_rsrc_t r = __builtin_amdgcn_make_buffer_rsrc((void*)base, 0, t < NSTEP ? PROJ_BYTES : 0u, 0x00020000);
  return __builtin_amdgcn_raw_buffer_load_b128(r, x.voff_s, (t & 31) * 128 + i * (8 * SKV * 2), 0);
}

__global__ void __launch_bounds__(256, 2)
linformer_decode_kernel(const uint16_t* __restrict__ q, const uint16_t* __restrict__ k, const uint16_t* __restrict__ v,
                        const uint16_t* __restrict__ e, const uint16_t* __restrict__ f, uint16_t* __restrict__ out) {
  extern __shared__ __attribute__((aligned(16))) char lds[];
  float* lds_s = (float*)(lds + RING);
  float* lds_p = lds_s + 4 * RK;
  float* lds_o = lds_p + 4 * RK;
  int* prog = (int*)(lds + RING + 4096);

  const int wave = __builtin_amdgcn_readfirstlane(threadIdx.x >> 6);
  const int hsel = WMAP == 0 ? (wave >> 1) : (wave & 1), par = WMAP == 0 ? (wave & 1) : (wave >> 1);
  const int head = HMAP == 0 ? blockIdx.x * 2 + hsel : HMAP == 1 ? blockIdx.x + 256 * hsel : ((blockIdx.x & 7) | ((blockIdx.x >> 3) << 4)) + 8 * hsel;
  const int lane = threadIdx.x & 63;
  const int g = lane >> 4, c = lane & 15;
  Ctx x;
  x.kb = (const char*)k + (size_t)head * HEAD_BYTES;
  x.vb = (const char*)v + (size_t)head * HEAD_BYTES;
  x.e = (const char*)e;
  x.f = (const char*)f;
  x.voff_d = par * (32 * DH * 2) + g * (8 * DH * 2) + c * 16;
  x.bx = BX * hsel * (64 * DH * 2);
  const unsigned rx = BX * hsel * 8192;
  x.voff_s = (16 * wave + (lane >> 3)) * (SKV * 2) + (lane & 7) * 16;
  const unsigned wo0 = (16 * wave + (lane >> 3)) * 128 + (((lane & 7) ^ g) << 4);
  const unsigned wo1 = (16 * wave + 8 + (lane >> 3)) * 128 + (((lane & 7) ^ (4 + g)) << 4);
  const unsigned ro = c * 128 + (((4 * par + g) ^ (c >> 1)) << 4);

  Tile t0, t1;
  u32x4 pa[4], st[2];
#pragma unroll
  for (int i = 0; i < WN; ++i) t0.d[i] = load_kv(x, 0, i);
  SB();
  u32x4 qv = *reinterpret_cast<const u32x4*>(q + head * DH + c * 8);
  u32x4 s0[SD][2];
#pragma unroll
  for (int j = 0; j < SD; ++j) s0[j][0] = load_pj(x, j, 0), s0[j][1] = load_pj(x, j, 1);
  st[0] = load_pj(x, SD, 0), st[1] = load_pj(x, SD, 1);
  SB();
  unsigned pfv;
  {
    const int j = blockIdx.x * 4 + wave;
    __amdgpu_buffer_rsrc_t r = __builtin_amdgcn_make_buffer_rsrc((void*)(j < 512 ? x.e : x.f), 0, PROJ_BYTES, 0x00020000);
    pfv = __builtin_amdgcn_raw_buffer_load_b32(r, lane < 8 ? lane * 64 : 0x7fffffff, (j & 511) * 512, 0);
  }
  SB();
#define PSLOT(kk)                                                        \
  {                                                                      \
    VMW(kk < WN ? WN - 1 + 2 * SD + 4 : WN - 1);                         \
    if (kk < CS) t0.d[(WN + kk) & 7] = load_kv(x, 0, WN + kk);           \
    else t1.d[(kk - CS) & 7] = load_kv(x, 1, kk - CS);                   \
    SB();                                                                \
    if (kk == (WN < 7 ? WN : 7)) {                                       \
      asm volatile("" : "+v"(qv) : "v"(pfv));                            \
      _Pragma("unroll") for (int j = 0; j < SD; ++j) {                   \
        *reinterpret_cast<u32x4*>(lds + j * 8192 + wo0) = s0[j][0];      \
        *reinterpret_cast<u32x4*>(lds + j * 8192 + wo1) = s0[j][1];      \
      }                                                                  \
      prog[wave] = 0;                                                    \
      SB();                                                              \
    }                                                                    \
  }
  PSLOT(0) PSLOT(1) PSLOT(2) PSLOT(3) PSLOT(4) PSLOT(5) PSLOT(6) PSLOT(7)
#undef PSLOT
  f32x4 acc[4][8];
#pragma unroll
  for (int b = 0; b < 4; ++b)
#pragma unroll
    for (int j = 0; j < 8; ++j) acc[b][j] = f32x4{0.f, 0.f, 0.f, 0.f};
  CB();
  LGW0();
  __builtin_amdgcn_s_barrier();
  CB();
#pragma unroll
  for (int b = 0; b < 4; ++b) pa[b] = *reinterpret_cast<const u32x4*>(lds + rx + b * 2048 + ro);
  SB();

  unsigned wslot = (SD % SR) * 8192, rslot = 8192;
  f32x4 pr[4];
  const unsigned prog_a = RING + 4096 + 4 * (lane & 3), prog_w = RING + 4096 + 4 * wave;

#define TR_STEP(HALF, CTRL, BIT)                                              \
  _Pragma("unroll") for (int h = 0; h < (HALF); ++h) {                        \
    const float lo_ = xs[h], hi_ = xs[h + (HALF)];                            \
    const float t1_ = lo_ + dpp<CTRL>(lo_), t2_ = hi_ + dpp<CTRL>(hi_);       \
    xs[h] = (c & (BIT)) ? t2_ : t1_;                                          \
  }
#define STEP(TC, TN, LAST) SLOT(TC, TN, LAST, 0) SLOT(TC, TN, LAST, 1) SLOT(TC, TN, LAST, 2) SLOT(TC, TN, LAST, 3) \
  SLOT(TC, TN, LAST, 4) SLOT(TC, TN, LAST, 5) SLOT(TC, TN, LAST, 6) SLOT(TC, TN, LAST, 7)
#define SLOT(TC, TN, LAST, kk)                                                                \
  {                                                                                           \
    if (kk <= CS) {                                                                           \
      if (kk == 0) compute_cols<0, 8 / (CS + 1) + (8 % (CS + 1) > 0)>(TC, pa, acc);          \
      if (kk == 1) compute_cols<8 / (CS + 1) + (8 % (CS + 1) > 0), CS == 1 ? 8 : CS == 2 ? 6 : 4>(TC, pa, acc); \
      if (kk == 2) compute_cols<CS == 2 ? 6 : 4, CS == 2 ? 8 : 6>(TC, pa, acc);              \
      if (kk == 3) compute_cols<6, 8>(TC, pa, acc);                                           \
      SB();                                                                                   \
    }                                                                                         \
    if (LAST == 0 || (LAST == 1 && kk < CS)) {                                                \
      VMW(vm_at(kk));                                                                         \
      if (kk < CS) TN.d[(WN + kk) & 7] = load_kv(x, t + 1, WN + kk);                          \
      else TC.d[(kk - CS) & 7] = load_kv(x, t + 2, kk - CS);                                  \
      SB();                                                                                   \
    }                                                                                         \
    if (kk == PS && LAST < 2) {                                                               \
      int pg;                                                                                 \
      asm volatile(                                                                           \
          "1:\n\tds_read_b32 %0, %1\n\ts_waitcnt lgkmcnt(0)\n\t"                              \
          "v_cmp_gt_i32 vcc, %2, %0\n\ts_nop 1\n\ts_cbranch_vccz 2f\n\t"                      \
          "s_sleep 1\n\ts_branch 1b\n2:"                                                      \
          : "=&v"(pg) : "v"(prog_a), "s"(t - LAG) : "memory", "vcc");                         \
      if (LAST == 0) {                                                                        \
        *reinterpret_cast<u32x4*>(lds + wslot + wo0) = st[0];                                 \
        *reinterpret_cast<u32x4*>(lds + wslot + wo1) = st[1];                                 \
      }                                                                                       \
      _Pragma("unroll") for (int b = 0; b < 4; ++b)                                           \
        pa[b] = *reinterpret_cast<const u32x4*>(lds + (rslot ^ rx) + b * 2048 + ro);                 \
      asm volatile("ds_write_b32 %0, %1" :: "v"(prog_w), "v"(t + 1) : "memory");              \
      wslot = wslot == (SR - 1) * 8192 ? 0 : wslot + 8192;                                    \
      rslot = rslot == (SR - 1) * 8192 ? 0 : rslot + 8192;                                    \
      SB();                                                                                   \
      if (LAST == 0) st[0] = load_pj(x, t + SD + 1, 0), st[1] = load_pj(x, t + SD + 1, 1);    \
      SB();                                                                                   \
    }                                                                                         \
  }

#pragma unroll 1
  for (int tt = 0; tt < NSTEP - 2; tt += 2) {
    asm volatile("" : "+v"(x.voff_d));
    int t = tt;
    STEP(t0, t1, 0)
    t = tt + 1;
    STEP(t1, t0, 0)
    if (tt == 30) {
      float qf[8], xs[16];
#pragma unroll
      for (int j = 0; j < 4; ++j) {
        qf[2 * j] = __uint_as_float(qv[j] << 16);
        qf[2 * j + 1] = __uint_as_float(qv[j] & 0xffff0000u);
      }
#pragma unroll
      for (int b = 0; b < 4; ++b)
#pragma unroll
        for (int m = 0; m < 4; ++m) {
          float a = 0.f;
#pragma unroll
          for (int j = 0; j < 8; ++j) a = fmaf(qf[j], acc[b][j][m], a);
          xs[b * 4 + m] = a;
        }
      TR_STEP(8, XOR8, 8) TR_STEP(4, XOR7, 4) TR_STEP(2, XOR2, 2) TR_STEP(1, XOR1, 1)
      lds_s[wave * RK + 16 * (c >> 2) + 4 * g + (c & 3)] = xs[0];
#pragma unroll
      for (int b = 0; b < 4; ++b)
#pragma unroll
        for (int j = 0; j < 8; ++j) acc[b][j] = f32x4{0.f, 0.f, 0.f, 0.f};
      CB();
    }
  }
  {
    int t = NSTEP - 2;
    STEP(t0, t1, 1)
    const int w0 = WMAP == 0 ? (wave & 2) : (wave & 1), w1 = WMAP == 0 ? w0 + 1 : w0 + 2;
    float sc = (lds_s[w0 * RK + lane] + lds_s[w1 * RK + lane]) * 0.08838834764831845f;
    float mx = sc;
    mx = fmaxf(mx, dpp<XOR1>(mx));
    mx = fmaxf(mx, dpp<XOR2>(mx));
    mx = fmaxf(mx, dpp<ROR4>(mx));
    mx = fmaxf(mx, dpp<XOR8>(mx));
    mx = xrow_max(mx);
    const float pe = __expf(sc - mx);
    float sum = pe;
    sum += dpp<XOR1>(sum);
    sum += dpp<XOR2>(sum);
    sum += dpp<ROR4>(sum);
    sum += dpp<XOR8>(sum);
    sum = xrow_sum(sum);
    lds_p[wave * RK + lane] = pe / sum;
    CB();
#pragma unroll
    for (int b = 0; b < 4; ++b) pr[b] = *reinterpret_cast<const f32x4*>(lds_p + wave * RK + 16 * b + 4 * g);
    SB();
    t = NSTEP - 1;
    STEP(t1, t0, 2)
  }
  float o[8];
#pragma unroll
  for (int j = 0; j < 8; ++j) {
    float a = 0.f;
#pragma unroll
    for (int b = 0; b < 4; ++b)
#pragma unroll
      for (int m = 0; m < 4; ++m) a = fmaf(pr[b][m], acc[b][j][m], a);
    o[j] = xrow_sum(a);
  }
  if (g == 0) {
    *reinterpret_cast<f32x4*>(lds_o + wave * DH + 8 * c) = f32x4{o[0], o[1], o[2], o[3]};
    *reinterpret_cast<f32x4*>(lds_o + wave * DH + 8 * c + 4) = f32x4{o[4], o[5], o[6], o[7]};
  }
  __syncthreads();
  if (par == 0) {
    const int w1 = WMAP == 0 ? wave + 1 : wave + 2;
    const int d0 = 2 * lane;
    float r0 = lds_o[wave * DH + d0] + lds_o[w1 * DH + d0];
    float r1 = lds_o[wave * DH + d0 + 1] + lds_o[w1 * DH + d0 + 1];
    __hip_bfloat162 pk;
    pk.x = __float2bfloat16(r0);
    pk.y = __float2bfloat16(r1);
    *reinterpret_cast<__hip_bfloat162*>(out + head * DH + d0) = pk;
  }
#undef STEP
#undef SLOT
#undef TR_STEP
}

extern "C" void kernel_launch(void* const* d_in, const int* in_sizes, int n_in, void* d_out, int out_size, void* d_ws,
                              size_t ws_size, hipStream_t stream) {
  const uint16_t* q = (const uint16_t*)d_in[0];
  const uint16_t* k = (const uint16_t*)d_in[1];
  const uint16_t* v = (const uint16_t*)d_in[2];
  const uint16_t* e = (const uint16_t*)d_in[3];
  const uint16_t* f = (const uint16_t*)d_in[4];
  (void)hipFuncSetAttribute((const void*)linformer_decode_kernel, hipFuncAttributeMaxDynamicSharedMemorySize, LDS_BYTES);
  linformer_decode_kernel<<<NHEAD / 2, 256, LDS_BYTES, stream>>>(q, k, v, e, f, (uint16_t*)d_out);
}
```

```cpp
#include <hip/hip_runtime.h>
#include <hip/hip_bf16.h>
#include <stdint.h>

typedef __attribute__((ext_vector_type(4))) unsigned u32x4;
typedef __attribute__((ext_vector_type(8))) __bf16 bf16x8;
typedef __attribute__((ext_vector_type(4))) float f32x4;
typedef __attribute__((ext_vector_type(2))) float f32x2;
typedef __attribute__((ext_vector_type(2))) __bf16 bf16x2;

#define SB() __builtin_amdgcn_sched_barrier(0)
#define CB() asm volatile("" ::: "memory")
#define VMW(n) __builtin_amdgcn_s_waitcnt(((n) & 15) | (7 << 4) | (15 << 8) | (((n) >> 4) << 14))
#define LGW0() __builtin_amdgcn_s_waitcnt(15 | (7 << 4) | (0 << 8) | (3 << 14))

constexpr int NHEAD = 512, SKV = 2048, DH = 128, RK = 64;
constexpr unsigned HEAD_BYTES = SKV * DH * 2;
constexpr unsigned PROJ_BYTES = RK * SKV * 2;
constexpr int NSTEP = 64;
constexpr int WN = 7;
constexpr int N0 = 6;
constexpr int SD = 4;
constexpr int SR = 12;
constexpr int PS = 3;
constexpr int LAG = 1;
constexpr int TAU = 219;
constexpr int TOFF = 220;
constexpr int GN = 100;
constexpr int RING = SR * 8192;
constexpr int LDS_BYTES = RING + 4096 + 64;

constexpr int vm_at(int k, int w) {
  int n = w - 1;
  for (int i = 1; i <= w; ++i)
    if (((k - i) & 7) == PS) n += 2;
  return n;
}
constexpr int thr_at(int k, int w) {
  const int rev = ((k & 1) << 2) | (k & 2) | ((k >> 2) & 1);
  return GN * (2 * rev + 1) + (w - 6) * GN * 16;
}

template <int CTRL>
__device__ __forceinline__ float dpp(float x) {
  return __builtin_bit_cast(float, __builtin_amdgcn_mov_dpp(__builtin_bit_cast(int, x), CTRL, 0xf, 0xf, true));
}
constexpr int XOR1 = 0xB1, XOR2 = 0x4E, XOR8 = 0x128, ROR4 = 0x124;

__device__ __forceinline__ float swap16_add(float a, float b) {
  auto s = __builtin_amdgcn_permlane16_swap(__float_as_uint(a), __float_as_uint(b), false, false);
  return __uint_as_float(s[0]) + __uint_as_float(s[1]);
}
__device__ __forceinline__ float swap32_add(float a, float b) {
  auto s = __builtin_amdgcn_permlane32_swap(__float_as_uint(a), __float_as_uint(b), false, false);
  return __uint_as_float(s[0]) + __uint_as_float(s[1]);
}
__device__ __forceinline__ float xrow_max(float x) {
  auto s = __builtin_amdgcn_permlane16_swap(__float_as_uint(x), __float_as_uint(x), false, false);
  x = fmaxf(__uint_as_float(s[0]), __uint_as_float(s[1]));
  auto t = __builtin_amdgcn_permlane32_swap(__float_as_uint(x), __float_as_uint(x), false, false);
  return fmaxf(__uint_as_float(t[0]), __uint_as_float(t[1]));
}
__device__ __forceinline__ float row_sum(float x) {
  x += dpp<XOR1>(x);
  x += dpp<XOR2>(x);
  x += dpp<ROR4>(x);
  return x + dpp<XOR8>(x);
}
__device__ __forceinline__ int fresh_lane() {
  int l;
  asm volatile("v_mbcnt_lo_u32_b32 %0, -1, 0\n\tv_mbcnt_hi_u32_b32 %0, -1, %0" : "=v"(l));
  return l;
}
__device__ __forceinline__ float bf_at(const u32x4& x, int i) {
  return __uint_as_float((i & 1) ? (x[i >> 1] & 0xffff0000u) : (x[i >> 1] << 16));
}

struct Tile {
  u32x4 d[8];
};
__device__ __forceinline__ unsigned col_pair(const Tile& t, int m, int j) {
  return __builtin_amdgcn_perm(t.d[2 * m + 1][j >> 1], t.d[2 * m][j >> 1], (j & 1) ? 0x07060302u : 0x05040100u);
}

template <int J0, int J1>
__device__ __forceinline__ void compute_cols(const Tile& t, const u32x4 (&p)[4], f32x4 (&acc)[4][8]) {
#pragma unroll
  for (int j = J0; j < J1; ++j) {
    u32x4 bf;
#pragma unroll
    for (int m = 0; m < 4; ++m) bf[m] = col_pair(t, m, j);
    const bf16x8 B = __builtin_bit_cast(bf16x8, bf);
#pragma unroll
    for (int b = 0; b < 4; ++b)
      acc[b][j] = __builtin_amdgcn_mfma_f32_16x16x32_bf16(__builtin_bit_cast(bf16x8, p[b]), B, acc[b][j], 0, 0, 0);
  }
}

struct Ctx {
  const char *kb, *vb, *e, *f;
  unsigned voff_d, voff_s, bx;
};
__device__ __forceinline__ u32x4 load_kv(const Ctx& x, int t, int i) {
  const char* base = t < 32 ? x.kb : x.vb;
  __amdgpu_buffer_rsrc_t r = __builtin_amdgcn_make_buffer_rsrc((void*)base, 0, HEAD_BYTES, 0x00020000);
  return __builtin_amdgcn_raw_buffer_load_b128(r, x.voff_d + i * (DH * 2), ((t & 31) * (64 * DH * 2)) ^ x.bx, 2);
}
__device__ __forceinline__ u32x4 load_pj(const Ctx& x, int t, int i) {
  const char* base = t < 32 ? x.e : x.f;
  __amdgpu_buffer_rsrc_t r = __builtin_amdgcn_make_buffer_rsrc((void*)base, 0, t < NSTEP ? PROJ_BYTES : 0u, 0x00020000);
  return __builtin_amdgcn_raw_buffer_load_b128(r, x.voff_s, (t & 31) * 128 + i * (8 * SKV * 2), 0);
}

__global__ void __launch_bounds__(256, 2)
linformer_decode_kernel(const uint16_t* __restrict__ q, const uint16_t* k, const uint16_t* v, const uint16_t* e,
                        const uint16_t* f, uint16_t* __restrict__ out) {
  const unsigned long long ts0 = __builtin_amdgcn_s_memrealtime();
  extern __shared__ __attribute__((aligned(16))) char lds[];
  float* lds_s = (float*)(lds + RING);
  float* lds_p = lds_s + 4 * RK;
  float* lds_o = lds_p + 4 * RK;
  int* prog = (int*)(lds + RING + 4096);

  const int wave = __builtin_amdgcn_readfirstlane(threadIdx.x >> 6);
  const int hsel = wave >> 1, par = wave & 1;
  const int head = blockIdx.x * 2 + hsel;
  const int lane = threadIdx.x & 63;
  const int g = lane >> 4, c = lane & 15;
  Ctx x;
  x.kb = (const char*)k + (size_t)head * HEAD_BYTES;
  x.vb = (const char*)v + (size_t)head * HEAD_BYTES;
  x.e = (const char*)e;
  x.f = (const char*)f;
  x.voff_d = par * (32 * DH * 2) + g * (8 * DH * 2) + c * 16;
  x.bx = hsel * (64 * DH * 2);
  const unsigned rx = hsel * 8192;
  x.voff_s = (16 * wave + (lane >> 3)) * (SKV * 2) + (lane & 7) * 16;
  const unsigned wo0 = (16 * wave + (lane >> 3)) * 128 + (((lane & 7) ^ g) << 4);
  const unsigned wo1 = (16 * wave + 8 + (lane >> 3)) * 128 + (((lane & 7) ^ (4 + g)) << 4);
  const unsigned ro = c * 128 + (((4 * par + g) ^ (c >> 1)) << 4);

  Tile t0, t1;
  u32x4 pa[4], st[2];
#pragma unroll
  for (int i = 0; i < N0; ++i) t0.d[i] = load_kv(x, 0, i);
  SB();
  u32x4 qv = *reinterpret_cast<const u32x4*>(q + head * DH + c * 8);
  u32x4 s0[SD][2];
#pragma unroll
  for (int j = 0; j < SD; ++j) s0[j][0] = load_pj(x, j, 0), s0[j][1] = load_pj(x, j, 1);
  st[0] = load_pj(x, SD, 0), st[1] = load_pj(x, SD, 1);
  SB();
  unsigned pfv;
  {
    const int j = blockIdx.x * 4 + wave;
    __amdgpu_buffer_rsrc_t r = __builtin_amdgcn_make_buffer_rsrc((void*)(j < 512 ? x.e : x.f), 0, PROJ_BYTES, 0x00020000);
    pfv = __builtin_amdgcn_raw_buffer_load_b32(r, lane < 4 ? lane * 128 : 0x7fffffff, (j & 511) * 512, 0);
  }
  SB();
  f32x4 acc[4][8];
#pragma unroll
  for (int b = 0; b < 4; ++b) {
#pragma unroll
    for (int j = 0; j < 8; ++j) acc[b][j] = f32x4{0.f, 0.f, 0.f, 0.f};
    asm volatile("" : "+v"(acc[b][0]), "+v"(acc[b][1]), "+v"(acc[b][2]), "+v"(acc[b][3]), "+v"(acc[b][4]),
                      "+v"(acc[b][5]), "+v"(acc[b][6]), "+v"(acc[b][7]));
  }
  SB();
#define PSLOT(kk)                                                   \
  {                                                                 \
    VMW(kk < 2 * N0 ? N0 - 1 + 2 * SD + 4 : N0 - 1);                \
    if (kk < 8) t0.d[kk & 7] = load_kv(x, 0, kk);                   \
    else t1.d[kk & 7] = load_kv(x, 1, kk - 8);                      \
    SB();                                                           \
    if (kk == 2 * N0) {                                             \
      asm volatile("" : "+v"(qv));                                  \
      asm volatile("" ::"v"(pfv));                                  \
      _Pragma("unroll") for (int j = 0; j < SD; ++j) {              \
        *reinterpret_cast<u32x4*>(lds + j * 8192 + wo0) = s0[j][0]; \
        *reinterpret_cast<u32x4*>(lds + j * 8192 + wo1) = s0[j][1]; \
      }                                                             \
      prog[wave] = 0;                                               \
      SB();                                                         \
    }                                                               \
  }
  PSLOT(6) PSLOT(7) PSLOT(8) PSLOT(9) PSLOT(10) PSLOT(11) PSLOT(12) PSLOT(13) PSLOT(14)
#undef PSLOT
  CB();
  LGW0();
  __builtin_amdgcn_s_barrier();
  CB();
#pragma unroll
  for (int b = 0; b < 4; ++b) pa[b] = *reinterpret_cast<const u32x4*>(lds + rx + b * 2048 + ro);
  SB();

  unsigned wslot = SD * 8192, rslot = 8192;
  unsigned sched = ((unsigned)ts0 + TOFF) * 16u + (8 + WN) * TAU, now = 0;
  const unsigned prog_a = RING + 4096 + 4 * (lane & 3), prog_w = RING + 4096 + 4 * wave;

  auto scores = [&](f32x4(&a)[8], int b) {
    const int l = fresh_lane();
    asm volatile("" : "+v"(qv));
    f32x2 lo = {0.f, 0.f}, hi = {0.f, 0.f};
#pragma unroll
    for (int j = 0; j < 8; ++j) {
      const f32x2 q2 = {bf_at(qv, j), bf_at(qv, j)};
      lo = __builtin_elementwise_fma(q2, f32x2{a[j][0], a[j][1]}, lo);
      hi = __builtin_elementwise_fma(q2, f32x2{a[j][2], a[j][3]}, hi);
      a[j] = f32x4{0.f, 0.f, 0.f, 0.f};
    }
    const float u0 = lo[0] + dpp<XOR2>(lo[0]), u2 = hi[0] + dpp<XOR2>(hi[0]);
    const float u1 = lo[1] + dpp<XOR2>(lo[1]), u3 = hi[1] + dpp<XOR2>(hi[1]);
    const float e0 = (l & 2) ? u2 : u0, e1 = (l & 2) ? u3 : u1;
    const float w0 = e0 + dpp<XOR1>(e0), w1 = e1 + dpp<XOR1>(e1);
    float r = (l & 1) ? w1 : w0;
    r += dpp<ROR4>(r);
    r += dpp<XOR8>(r);
    lds_s[wave * RK + 16 * b + 4 * (l >> 4) + (l & 3)] = r;
    CB();
  };
  auto softmax = [&]() {
    const int l = fresh_lane();
    const float sc = (lds_s[(wave & 2) * RK + l] + lds_s[((wave & 2) + 1) * RK + l]) * 0.08838834764831845f;
    float mx = sc;
    mx = fmaxf(mx, dpp<XOR1>(mx));
    mx = fmaxf(mx, dpp<XOR2>(mx));
    mx = fmaxf(mx, dpp<ROR4>(mx));
    mx = fmaxf(mx, dpp<XOR8>(mx));
    mx = xrow_max(mx);
    const float pe = __expf(sc - mx);
    float sum = row_sum(pe);
    sum = swap16_add(sum, sum);
    sum = swap32_add(sum, sum);
    lds_p[wave * RK + l] = pe / sum;
    CB();
  };

#define STEP(TC, TN, ODD)                                                                                   \
  SLOT(TC, TN, ODD, 0) SLOT(TC, TN, ODD, 1) SLOT(TC, TN, ODD, 2) SLOT(TC, TN, ODD, 3) SLOT(TC, TN, ODD, 4) \
  SLOT(TC, TN, ODD, 5) SLOT(TC, TN, ODD, 6) SLOT(TC, TN, ODD, 7)
#define SLOT(TC, TN, ODD, kk)                                                                          \
  {                                                                                                    \
    if (kk == 0) {                                                                                     \
      now = (unsigned)__builtin_amdgcn_s_memrealtime();                                                \
      SB();                                                                                            \
      compute_cols<0, 4>(TC, pa, acc);                                                                 \
    }                                                                                                  \
    if (kk == 1) compute_cols<4, 8>(TC, pa, acc);                                                      \
    SB();                                                                                              \
    VMW(vm_at(kk, 7));                                                                                 \
    asm volatile(                                                                                      \
        "s_cmp_lt_i32 %0, %1\n\ts_cbranch_scc0 1f\n\ts_waitcnt vmcnt(%3)\n\t"                          \
        "s_cmp_lt_i32 %0, %2\n\ts_cbranch_scc0 1f\n\ts_waitcnt vmcnt(%4)\n1:" ::"s"(now * 16u - sched), \
        "n"(thr_at(kk, 6)), "n"(thr_at(kk, 5)), "n"(vm_at(kk, 6)), "n"(vm_at(kk, 5))                   \
        : "scc");                                                                                      \
    if (kk == 7) sched += 8 * TAU;                                                                     \
    if (kk == 0) TN.d[7] = load_kv(x, t + 1, 7);                                                       \
    else TC.d[(kk - 1) & 7] = load_kv(x, t + 2, kk - 1);                                               \
    SB();                                                                                              \
    if (kk == PS) {                                                                                    \
      int pg;                                                                                          \
      asm volatile(                                                                                    \
          "1:\n\tds_read_b32 %0, %1\n\ts_waitcnt lgkmcnt(0)\n\t"                                       \
          "v_cmp_gt_i32 vcc, %2, %0\n\ts_nop 1\n\ts_cbranch_vccz 2f\n\t"                               \
          "s_sleep 1\n\ts_branch 1b\n2:"                                                               \
          : "=&v"(pg)                                                                                  \
          : "v"(prog_a), "s"(t - LAG)                                                                  \
          : "memory", "vcc");                                                                          \
      *reinterpret_cast<u32x4*>(lds + wslot + wo0) = st[0];                                            \
      *reinterpret_cast<u32x4*>(lds + wslot + wo1) = st[1];                                            \
      _Pragma("unroll") for (int b = 0; b < 4; ++b)                                                    \
          pa[b] = *reinterpret_cast<const u32x4*>(lds + (rslot ^ rx) + b * 2048 + ro);                 \
      asm volatile("ds_write_b32 %0, %1" ::"v"(prog_w), "v"(t + 1) : "memory");                        \
      wslot = wslot == (SR - 1) * 8192 ? 0 : wslot + 8192;                                             \
      rslot = rslot == (SR - 1) * 8192 ? 0 : rslot + 8192;                                             \
      SB();                                                                                            \
      st[0] = load_pj(x, t + SD + 1, 0), st[1] = load_pj(x, t + SD + 1, 1);                            \
      SB();                                                                                            \
    }                                                                                                  \
    if (ODD && (kk == 2 || (kk >= 4 && kk <= 6))) {                  \
      if (t == 31) scores(acc[kk == 2 ? 0 : (kk - 3) & 3], kk == 2 ? 0 : kk - 3);                      \
      asm volatile("" : "+v"(x.voff_d));                                                               \
      SB();                                                                                            \
    }                                                                                                  \
    if (!ODD && kk == 4) {         \
      if (t == 36) softmax();                                                                          \
      asm volatile("" : "+v"(x.voff_d));                                                               \
      SB();                                                                                            \
    }                                                                                                  \
  }

#pragma unroll 1
  for (int tt = 0; tt < NSTEP - 2; tt += 2) {
    asm volatile("" : "+v"(x.voff_d));
    int t = tt;
    STEP(t0, t1, false)
    t = tt + 1;
    STEP(t1, t0, true)
  }
#undef STEP
#undef SLOT

  asm volatile("" : "+v"(x.voff_d));
  const int l2 = fresh_lane(), g2 = l2 >> 4, c2 = l2 & 15;
  t1.d[7] = load_kv(x, NSTEP - 1, 7);
  SB();
  u32x4 pw[4];
  f32x4 pr[4];
  float pc[4], y[8], o[8];
  compute_cols<0, 6>(t0, pa, acc);
  SB();
#pragma unroll
  for (int b = 0; b < 4; ++b) {
    pw[b] = *reinterpret_cast<const u32x4*>(lds + (rslot ^ rx) + b * 2048 + ro);
    pc[b] = lds_p[wave * RK + 16 * b + c2];
  }
  SB();
  compute_cols<6, 8>(t0, pa, acc);
  SB();
#pragma unroll
  for (int b = 0; b < 4; ++b) pr[b] = *reinterpret_cast<const f32x4*>(lds_p + wave * RK + 16 * b + 4 * g2);
#pragma unroll
  for (int m = 0; m < 8; ++m) {
    float a = 0.f;
#pragma unroll
    for (int b = 0; b < 4; ++b) a = fmaf(pc[b], bf_at(pw[b], m), a);
    a = row_sum(a);
    const float hi = __uint_as_float(__float_as_uint(a) & 0xffff0000u);
    y[m] = (c2 & 1) ? a - hi : hi;
  }
  const bf16x8 wa = {(__bf16)y[0], (__bf16)y[1], (__bf16)y[2], (__bf16)y[3],
                     (__bf16)y[4], (__bf16)y[5], (__bf16)y[6], (__bf16)y[7]};
#pragma unroll
  for (int j = 0; j < 8; ++j) {
    float a = 0.f;
#pragma unroll
    for (int b = 0; b < 4; ++b)
#pragma unroll
      for (int m = 0; m < 4; ++m) a = fmaf(pr[b][m], acc[b][j][m], a);
    o[j] = a;
  }
  f32x2 rr;
  rr[0] = swap16_add(swap32_add(o[0], o[4]), swap32_add(o[2], o[6]));
  rr[1] = swap16_add(swap32_add(o[1], o[5]), swap32_add(o[3], o[7]));
  SB();
  u32x4 bf[8];
#pragma unroll
  for (int m = 0; m < 3; ++m)
#pragma unroll
    for (int j = 0; j < 8; ++j) bf[j][m] = col_pair(t1, m, j);
  SB();
  f32x4 d[8];
#pragma unroll
  for (int j = 0; j < 8; ++j) {
    bf[j][3] = col_pair(t1, 3, j);
    d[j] = __builtin_amdgcn_mfma_f32_16x16x32_bf16(wa, __builtin_bit_cast(bf16x8, bf[j]), f32x4{0.f, 0.f, 0.f, 0.f}, 0, 0, 0);
  }
  asm volatile("" : "+v"(d[0]), "+v"(d[1]), "+v"(d[2]), "+v"(d[3]), "+v"(d[4]), "+v"(d[5]), "+v"(d[6]), "+v"(d[7]));
  float z[8];
#pragma unroll
  for (int j = 0; j < 8; ++j) z[j] = d[j][0] + d[j][1];
  const float u0 = (g2 & 1) ? z[2] : z[0], u1 = (g2 & 1) ? z[6] : z[4];
  const float u2 = (g2 & 1) ? z[3] : z[1], u3 = (g2 & 1) ? z[7] : z[5];
  rr[0] += (g2 & 2) ? u1 : u0;
  rr[1] += (g2 & 2) ? u3 : u2;

  if (par == 1) *reinterpret_cast<f32x2*>(lds_o + hsel * DH + 2 * l2) = rr;
  __syncthreads();
  if (par == 0) {
    rr += *reinterpret_cast<const f32x2*>(lds_o + hsel * DH + 2 * l2);
    const bf16x2 pk = {(__bf16)rr[0], (__bf16)rr[1]};
    *reinterpret_cast<bf16x2*>(out + head * DH + 8 * c2 + 2 * g2) = pk;
  }
}

extern "C" void kernel_launch(void* const* d_in, const int* in_sizes, int n_in, void* d_out, int out_size, void* d_ws,
                              size_t ws_size, hipStream_t stream) {
  const uint16_t* q = (const uint16_t*)d_in[0];
  const uint16_t* k = (const uint16_t*)d_in[1];
  const uint16_t* v = (const uint16_t*)d_in[2];
  const uint16_t* e = (const uint16_t*)d_in[3];
  const uint16_t* f = (const uint16_t*)d_in[4];
  (void)hipFuncSetAttribute((const void*)linformer_decode_kernel, hipFuncAttributeMaxDynamicSharedMemorySize, LDS_BYTES);
  linformer_decode_kernel<<<NHEAD / 2, 256, LDS_BYTES, stream>>>(q, k, v, e, f, (uint16_t*)d_out);
}
```
